```python
import torch

def _softmax(x, axis=-1):
    return torch.softmax(x, dim=axis)
def _gelu(x):
    return torch.nn.functional.gelu(x, approximate="tanh")
def _silu(x):
    return torch.nn.functional.silu(x)
def _sigmoid(x):
    return torch.sigmoid(x)
def _layernorm(x, axis=-1, eps=1e-5):
    m = x.mean(dim=axis, keepdim=True)
    v = x.var(dim=axis, keepdim=True, unbiased=False)
    return (x - m) / torch.sqrt(v + eps)
def _scatter_add(tgt, idx, src, axis=0):
    return tgt.clone().index_add_(axis, idx, src)
def _segment_sum(src, seg, n):
    out = torch.zeros((n,) + src.shape[1:], dtype=src.dtype)
    return out.index_add_(0, seg, src)
def _pad(x, pw):
    flat = tuple(v for pair in reversed(pw) for v in pair)
    return torch.nn.functional.pad(x, flat)

kernel_name = 'recurrent_scan__sliding_local_dilated__dense_activation_squared_relu__prompt'

def setup_inputs():

    _w = (lambda a: (lambda t: t.to(torch.bfloat16) if t.is_floating_point() else t)(torch.as_tensor(a)))
    def _A_setup():
        B, S, D = 1, 2048, 640
        return {
            "x": _w((lambda *s: torch.randn(*s, dtype=torch.bfloat16) * 0.05)(B, S, D)),
            "wqkv": _w((lambda *s: torch.randn(*s, dtype=torch.bfloat16) * 0.05)(D, 3 * D)),
            "wo": _w((lambda *s: torch.randn(*s, dtype=torch.bfloat16) * 0.05)(D, D)),
        }
    _A_in = _A_setup()

    def _B_setup():
        B, S, D, F = 1, 2048, 640, 1536
        return {
            "x": _w((lambda *s: torch.randn(*s, dtype=torch.bfloat16) * 0.05)(B, S, D)),
            "w1": _w((lambda *s: torch.randn(*s, dtype=torch.bfloat16) * 0.05)(D, F)),
            "b1": _w((lambda *s: torch.randn(*s, dtype=torch.bfloat16) * 0.4)(F)),
            "w2": _w((lambda *s: torch.randn(*s, dtype=torch.bfloat16) * 0.05)(F, D)),
            "b2": _w((lambda *s: torch.randn(*s, dtype=torch.bfloat16) * 1.6)(D)),
        }
    _B_in = _B_setup()

    return {
        "A_x": _A_in["x"],
        "A_wqkv": _A_in["wqkv"],
        "A_wo": _A_in["wo"],
        "B_x": _B_in["x"],
        "B_w1": _B_in["w1"],
        "B_b1": _B_in["b1"],
        "B_w2": _B_in["w2"],
        "B_b2": _B_in["b2"],
    }

def reference(A_x, A_wqkv, A_wo, B_x, B_w1, B_b1, B_w2, B_b2):
    def _A_ref(x, wqkv, wo):
        B, S, D = x.shape
        H = 32; Dh = D // H
        W, dil = 8, 2
        qkv = (lambda x, *d: x.permute(*d))((x @ wqkv).reshape(B, S, 3, H, Dh), 2, 0, 3, 1, 4)
        q, k, v = qkv[0], qkv[1], qkv[2]
        scale = Dh ** -0.5
        pos = torch.arange(S)
        off = torch.arange(W) * dil
        idx = pos[:, None] - off[None, :]
        mask = idx >= 0
        idx_c = torch.where(mask, idx, 0).reshape(-1)
        kg = k[:, :, idx_c, :].reshape(B, H, S, W, Dh)
        vg = v[:, :, idx_c, :].reshape(B, H, S, W, Dh)
        logits = torch.einsum("bhsd,bhswd->bhsw", q, kg) * scale
        logits = torch.where(mask[None, None, :, :], logits, -1e9)
        attn = _softmax(logits, axis=-1)
        o = torch.einsum("bhsw,bhswd->bhsd", attn, vg)
        return (lambda x, *d: x.permute(*d))(o, 0, 2, 1, 3).reshape(B, S, D) @ wo

    def _B_ref(x, w1, b1, w2, b2):
        h = torch.einsum("bsd,df->bsf", x, w1) + b1
        zero = torch.zeros_like(h)
        r = torch.where(h > zero, h, zero)
        h = r * r
        out = torch.einsum("bsf,fd->bsd", h, w2) + b2
        return out

    x = A_x
    xn = _layernorm(x, axis=-1)
    s = _A_ref(xn, A_wqkv, A_wo)
    g = _silu(_B_ref(xn, B_w1, B_b1, B_w2, B_b2))
    return x + s * g
```

```cpp
#include <hip/hip_runtime.h>
#include <hip/hip_bf16.h>
typedef unsigned short u16;
__device__ __forceinline__ float bf2f(u16 b){ return __uint_as_float(((unsigned)b)<<16); }
__device__ __forceinline__ u16 f2bf(float f){ unsigned u=__float_as_uint(f); u += 0x7fffu + ((u>>16)&1u); return (u16)(u>>16); }
constexpr int S=2048,D=640,F=1536,H=32,DH=20;

__global__ void k_ln(const u16* x, u16* xn){
  int r=blockIdx.x; int t=threadIdx.x;
  float s=0; for(int i=t;i<D;i+=64) s+=bf2f(x[r*D+i]);
  for(int o=32;o;o>>=1) s+=__shfl_xor(s,o);
  float m=s/D; float v=0; for(int i=t;i<D;i+=64){float d=bf2f(x[r*D+i])-m; v+=d*d;}
  for(int o=32;o;o>>=1) v+=__shfl_xor(v,o);
  v/=D; float rs=rsqrtf(v+1e-5f);
  for(int i=t;i<D;i+=64) xn[r*D+i]=f2bf((bf2f(x[r*D+i])-m)*rs);
}
__global__ void k_gemm(const u16* A,const u16* B,const u16* bias,u16* C,float* Cf,int M,int N,int K,int mode){
  int n=blockIdx.x*64+threadIdx.x%64; int m=blockIdx.y*4+threadIdx.x/64;
  if(n>=N) return;
  float acc=0; for(int k=0;k<K;k++) acc+=bf2f(A[m*K+k])*bf2f(B[k*N+n]);
  if(mode==0) C[m*N+n]=f2bf(acc);
  else if(mode==1){ float h=bf2f(f2bf(bf2f(f2bf(acc))+bf2f(bias[n]))); h=h>0?h:0; C[m*N+n]=f2bf(h*h);}
  else if(mode==2){ Cf[m*N+n]=bf2f(f2bf(bf2f(f2bf(acc))+bf2f(bias[n]))); }
  else { Cf[m*N+n]=bf2f(f2bf(acc)); }
}
__global__ void k_attn(const u16* qkv,u16* o){
  int idx=blockIdx.x*blockDim.x+threadIdx.x; int s=idx/H,h=idx%H;
  float l[8]; float mx=-1e30f;
  for(int w=0;w<8;w++){ int j=s-2*w; float a=0; if(j>=0){ for(int d=0;d<DH;d++) a+=bf2f(qkv[s*1920+h*DH+d])*bf2f(qkv[j*1920+640+h*DH+d]); a*=0.22360679774997896f;} else a=-1e9f; l[w]=a; mx=fmaxf(mx,a);}
  float sum=0; for(int w=0;w<8;w++){ l[w]=__expf(l[w]-mx); sum+=l[w]; }
  for(int d=0;d<DH;d++){ float a=0; for(int w=0;w<8;w++){ int j=s-2*w; if(j<0) j=0; a+=l[w]/sum*bf2f(qkv[j*1920+1280+h*DH+d]); } o[s*D+h*DH+d]=f2bf(a);}
}
__global__ void k_fin(const u16* x,const float* sv,const float* f,u16* out){
  int i=blockIdx.x*blockDim.x+threadIdx.x; float g=f[i]/(1.f+__expf(-f[i])); out[i]=f2bf(bf2f(x[i])+sv[i]*g);
}
extern "C" void kernel_launch(void* const* d_in, const int* in_sizes, int n_in, void* d_out, int out_size, void* d_ws, size_t ws_size, hipStream_t stream){
  const u16* x=(const u16*)d_in[0]; const u16* wqkv=(const u16*)d_in[1]; const u16* wo=(const u16*)d_in[2];
  const u16* w1=(const u16*)d_in[4]; const u16* b1=(const u16*)d_in[5]; const u16* w2=(const u16*)d_in[6]; const u16* b2=(const u16*)d_in[7];
  char* ws=(char*)d_ws; u16* xn=(u16*)ws; u16* qkv=(u16*)(ws+(4<<20)); u16* o=(u16*)(ws+(16<<20)); u16* h=(u16*)(ws+(24<<20)); float* sv=(float*)(ws+(40<<20)); float* f=(float*)(ws+(56<<20));
  k_ln<<<S,64,0,stream>>>(x,xn);
  k_gemm<<<dim3(30,S/4),256,0,stream>>>(xn,wqkv,nullptr,qkv,nullptr,S,1920,D,0);
  k_attn<<<S*H/256,256,0,stream>>>(qkv,o);
  k_gemm<<<dim3(10,S/4),256,0,stream>>>(o,wo,nullptr,nullptr,sv,S,D,D,3);
  k_gemm<<<dim3(24,S/4),256,0,stream>>>(xn,w1,b1,h,nullptr,S,F,D,1);
  k_gemm<<<dim3(10,S/4),256,0,stream>>>(h,w2,b2,nullptr,f,S,D,F,2);
  k_fin<<<S*D/256,256,0,stream>>>(x,sv,f,(u16*)d_out);
}
```

```cpp
#include <hip/hip_runtime.h>
#include <stdint.h>

typedef unsigned short u16;
typedef unsigned long long u64;
typedef __bf16 bf8 __attribute__((ext_vector_type(8)));
typedef __bf16 bf4 __attribute__((ext_vector_type(4)));
typedef __bf16 bf2 __attribute__((ext_vector_type(2)));
typedef float f4 __attribute__((ext_vector_type(4)));
typedef float f2 __attribute__((ext_vector_type(2)));
typedef unsigned u4 __attribute__((ext_vector_type(4)));
typedef unsigned u2 __attribute__((ext_vector_type(2)));
#define LAS __attribute__((address_space(3)))
#define GAS __attribute__((address_space(1)))
#define DI __device__ __forceinline__

#define STAMPS 0

constexpr int SLOT = 49152;
constexpr int AUX = 3 * SLOT;
constexpr int AUX_ONES = AUX;
constexpr int AUX_NM = AUX + 256;
constexpr int AUX_R = AUX_NM + 576;
constexpr int AUX_CS = AUX_NM + 1152;
constexpr int AUX_TAG = AUX_CS + 1280;
constexpr int AUX_CNT = AUX_TAG + 8;
constexpr int AUX_DUMMY = AUX_TAG + 16;
constexpr int LDS_BYTES = AUX_DUMMY + 16;
constexpr int XOFF_A = 30720;
constexpr int XOFF_F = 24576;
constexpr int SC_REM = 0;
constexpr int SC_O = 16384;
constexpr int OPITCH = 176;

constexpr size_t WS_FLAG = 4096;
constexpr size_t WS_STAT = 16384;
constexpr size_t WS_STAMP = 65536;
constexpr size_t WS_ACT = 1 << 20;
constexpr int ACT_TB = 72 * 1024;

constexpr int RBF = 6, RBA = 4;

DI void bar() { asm volatile("s_waitcnt lgkmcnt(0)\n\ts_barrier" ::: "memory"); }
template <int N> DI void waitv() { asm volatile("s_waitcnt vmcnt(%0)" ::"i"(N) : "memory"); }
DI f4 mfma(bf8 a, bf8 b, f4 c) { return __builtin_amdgcn_mfma_f32_16x16x32_bf16(a, b, c, 0, 0, 0); }
DI unsigned rfl(unsigned v) { return __builtin_amdgcn_readfirstlane(v); }
DI void dma(unsigned ldsaddr, unsigned voff, __amdgpu_buffer_rsrc_t rs, unsigned soff) {
  unsigned keep;
  asm volatile("s_mov_b32 %0, m0\n\ts_mov_b32 m0, %1\n\ts_nop 0\n\tbuffer_load_dwordx4 %2, %3, %4 offen lds\n\ts_mov_b32 m0, %0"
               : "=&s"(keep) : "s"(ldsaddr), "v"(voff), "s"(rs), "s"(soff) : "memory");
}
DI void dma60(unsigned ldsaddr, unsigned voff, __amdgpu_buffer_rsrc_t rs, unsigned soff) {
  unsigned keep;
  asm volatile("s_mov_b32 %0, m0\n\ts_mov_b32 m0, %1\n\ts_mov_b32 exec_hi, 0x0fffffff\n\tbuffer_load_dwordx4 %2, %3, %4 offen lds\n\ts_mov_b32 exec_hi, -1\n\ts_mov_b32 m0, %0"
               : "=&s"(keep) : "s"(ldsaddr), "v"(voff), "s"(rs), "s"(soff) : "memory");
}
DI void mfma_ip(f4& c, bf8 a, bf8 b) { asm volatile("v_mfma_f32_16x16x32_bf16 %0, %1, %2, %0" : "+v"(c) : "v"(a), "v"(b)); }
DI bf8 ldx(LAS char* p) { return *(LAS bf8*)p; }
DI bf8 ldw(LAS char* p, int tstride) {
  bf4 a = __builtin_amdgcn_ds_read_tr16_b64_v4bf16((LAS bf4*)p);
  bf4 b = __builtin_amdgcn_ds_read_tr16_b64_v4bf16((LAS bf4*)(p + tstride));
  return __builtin_shufflevector(a, b, 0, 1, 2, 3, 4, 5, 6, 7);
}
DI float dot8(bf8 a, bf8 b, float c) {
  c = __builtin_amdgcn_fdot2_f32_bf16(__builtin_shufflevector(a, a, 0, 1), __builtin_shufflevector(b, b, 0, 1), c, false);
  c = __builtin_amdgcn_fdot2_f32_bf16(__builtin_shufflevector(a, a, 2, 3), __builtin_shufflevector(b, b, 2, 3), c, false);
  c = __builtin_amdgcn_fdot2_f32_bf16(__builtin_shufflevector(a, a, 4, 5), __builtin_shufflevector(b, b, 4, 5), c, false);
  c = __builtin_amdgcn_fdot2_f32_bf16(__builtin_shufflevector(a, a, 6, 7), __builtin_shufflevector(b, b, 6, 7), c, false);
  return c;
}
DI bf8 ones8() { u4 o = {0x3f803f80u, 0x3f803f80u, 0x3f803f80u, 0x3f803f80u}; return __builtin_bit_cast(bf8, o); }
DI unsigned pk2(float a, float b) { f2 v = {a, b}; return __builtin_bit_cast(unsigned, __builtin_convertvector(v, bf2)); }
DI u2 pk4(f4 v) { u2 r = {pk2(v[0], v[1]), pk2(v[2], v[3])}; return r; }
DI float bfr(float a) { return __builtin_bit_cast(float, pk2(a, 0.f) << 16); }
DI float lo16(unsigned u) { return __builtin_bit_cast(float, u << 16); }
DI float hi16(unsigned u) { return __builtin_bit_cast(float, u & 0xffff0000u); }
DI bf8 cat(u2 a, u2 b) { u4 r = {a[0], a[1], b[0], b[1]}; return __builtin_bit_cast(bf8, r); }
DI float asf(unsigned u) { return __builtin_bit_cast(float, u); }
DI unsigned asu(float f) { return __builtin_bit_cast(unsigned, f); }
DI float xg_max(float v) {
  auto a = __builtin_amdgcn_permlane16_swap(asu(v), asu(v), false, false);
  v = __builtin_elementwise_maximum(asf(a[0]), asf(a[1]));
  auto b = __builtin_amdgcn_permlane32_swap(asu(v), asu(v), false, false);
  return __builtin_elementwise_maximum(asf(b[0]), asf(b[1]));
}
DI float xg_sum(float v) {
  auto a = __builtin_amdgcn_permlane16_swap(asu(v), asu(v), false, false);
  v = asf(a[0]) + asf(a[1]);
  auto b = __builtin_amdgcn_permlane32_swap(asu(v), asu(v), false, false);
  return asf(b[0]) + asf(b[1]);
}
DI float red4(float a, float b, float c, float d) {
  auto u = __builtin_amdgcn_permlane16_swap(asu(a), asu(b), false, false);
  auto v = __builtin_amdgcn_permlane16_swap(asu(c), asu(d), false, false);
  float ab = asf(u[0]) + asf(u[1]), cd = asf(v[0]) + asf(v[1]);
  auto t = __builtin_amdgcn_permlane32_swap(asu(ab), asu(cd), false, false);
  return asf(t[0]) + asf(t[1]);
}
template <int CTRL> DI float dpp_add(float v) { return v + asf(__builtin_amdgcn_update_dpp(0u, asu(v), CTRL, 0xf, 0xf, true)); }
DI float fmx(float a, float b) { return __builtin_elementwise_maximum(a, b); }
#define PIN4(a, b, c, d) asm volatile("" : "+v"(a), "+v"(b), "+v"(c), "+v"(d))
#define PIN1(a) asm volatile("" : "+v"(a))
#define SCHED() __builtin_amdgcn_sched_barrier(0)
#define SGB(mask, n) __builtin_amdgcn_sched_group_barrier(mask, n, 0)
DI u64 ld_sc1(const u64* p) { return __hip_atomic_load((GAS u64*)p, __ATOMIC_RELAXED, __HIP_MEMORY_SCOPE_AGENT); }
DI void st_sc1(u64* p, u64 v) { __hip_atomic_store((GAS u64*)p, v, __ATOMIC_RELAXED, __HIP_MEMORY_SCOPE_AGENT); }
DI void poll(const u64* p, u64 tag) {
#pragma nounroll
  for (int it = 0; it < (1 << 16); ++it) {
    u64 v = ld_sc1(p);
    if (__all(v == tag)) break;
    __builtin_amdgcn_s_sleep(2);
  }
  __builtin_amdgcn_fence(__ATOMIC_ACQUIRE, "wavefront");
}
#define STAMP(i)
#define LSTAMP(i)

__global__ __launch_bounds__(768, 3) void fused(const u16* __restrict__ x, const u16* __restrict__ wqkv, const u16* __restrict__ wo,
                                                const u16* __restrict__ w1, const u16* __restrict__ b1, const u16* __restrict__ w2,
                                                const u16* __restrict__ b2, u16* __restrict__ out, char* __restrict__ ws) {
  extern __shared__ __attribute__((aligned(16))) char smem[];
  LAS char* lds = (LAS char*)smem;
  const unsigned ldsb = (unsigned)(size_t)lds;
  const int tid = threadIdx.x, lane = tid & 63, w = rfl(tid >> 6);
  const int b = blockIdx.x, xcd = b & 7, loc = b >> 3;
  const int rq = xcd >> 1, half = xcd & 1;
  const bool isattn = loc < 16;
  const int li = loc & 15;
  const int panel = 4 * rq + (li & 3);
  const int grp = 4 * half + (li >> 2);
  const int P0 = panel * 128;
#define ROT 0
  const int rot = ROT;
  int ctl, rb0;
  if (isattn) { if (li < 6) { ctl = li >> 1; rb0 = 24 + 4 * (li & 1); } else { ctl = 3 + (li - 6) / 5; rb0 = 12 + 4 * ((li - 6) % 5); } }
  else        { if (li < 12) { ctl = li >> 2; rb0 = 6 * (li & 3); } else { ctl = 3 + ((li - 12) >> 1); rb0 = 6 * (li & 1); } }
  const int ct = 5 * half + ctl;
  const int tb0 = rq * 32 + rb0;
  const int RB = isattn ? RBA : RBF;
  const int g = lane >> 4, c16 = lane & 15, q4 = (lane >> 2) & 3, p4 = lane & 3;
  u64* flags = (u64*)(ws + WS_FLAG);
  const u64* myflag = flags + ((lane & 8) ? (tb0 + RB - 1) >> 3 : tb0 >> 3) * 32 + (lane & 7);
  STAMP(0);

  if (w >= 8) {
    __builtin_amdgcn_s_setprio(3);
    const int lw = w - 8;
    const int rho = lane >> 3, cs8 = lane & 7, hh = lw >> 1, tt = lw & 1;
    auto rsx = __builtin_amdgcn_make_buffer_rsrc((void*)x, 0, 2048 * 1280, 0x00020000);
    auto rsw = isattn ? __builtin_amdgcn_make_buffer_rsrc((void*)wqkv, 0, 640 * 3840, 0x00020000)
                      : __builtin_amdgcn_make_buffer_rsrc((void*)w1, 0, 640 * 3072, 0x00020000);
    const unsigned swz = (cs8 ^ (rho & 6)) << 4;
    const unsigned vx = rho * (isattn ? 2560 : 1280) + ((cs8 ^ (rho >> 1) ^ (tt << 2)) << 4);
    unsigned vw;
    if (isattn) {
      int r2 = lane >= 30, cc = lane - 30 * r2, seg = (cc >= 10) + (cc >= 20);
      vw = (8 * hh + 2 * tt + r2) * 3840 + seg * 1120 + cc * 16 + grp * 160;
    } else {
      vw = (16 * hh + 4 * tt + (rho >> 2) * 8 + (rho & 3)) * 3072 + grp * 384 + swz;
    }
    unsigned xrow[5];
#pragma unroll
    for (int i = 0; i < 5; ++i) {
      int n = lw + 4 * i, m = n - 2;
      int ta = n < 2 ? P0 - 16 + n : P0 + 32 * (m >> 2) + 16 * (m & 1) + ((m >> 1) & 1);
      ta = ta < 0 ? 0 : ta;
      xrow[i] = (isattn ? ta : P0 + 8 * n) * 1280;
    }
    const bool ext = isattn && lw < 2;
    asm volatile("s_nop 4" ::: "memory");

    auto issue1a = [&](int t) {
      int kt = t + rot; kt = kt >= 10 ? kt - 10 : kt;
      unsigned sb = ldsb + (t % 3) * SLOT;
      unsigned sx = kt * 128;
      unsigned xb = sb + (isattn ? XOFF_A : XOFF_F) + lw * 1024;
#pragma unroll
      for (int i = 0; i < 4; ++i) dma(xb + i * 4096, vx, rsx, sx + xrow[i]);
      if (isattn) {
        unsigned sw = kt * (64 * 3840);
#pragma unroll
        for (int i = 0; i < 2; ++i) dma60(sb + lw * 960 + i * 3840, vw, rsw, sw + (32 * (i >> 2) + 16 * ((i >> 1) & 1) + 4 * (i & 1)) * 3840);
      } else {
        unsigned sw = kt * (64 * 3072);
#pragma unroll
        for (int i = 0; i < 2; ++i) dma(sb + lw * 1024 + i * 4096, vw, rsw, sw + (i / 3) * 32 * 3072 + (i % 3) * 128);
      }
    };
    auto issue1b = [&](int t) {
      int kt = t + rot; kt = kt >= 10 ? kt - 10 : kt;
      unsigned sb = ldsb + (t % 3) * SLOT;
      if (isattn) {
        unsigned sw = kt * (64 * 3840);
        if (ext) dma(sb + XOFF_A + lw * 1024 + 4 * 4096, vx, rsx, kt * 128 + xrow[4]);
#pragma unroll
        for (int i = 2; i < 8; ++i) dma60(sb + lw * 960 + i * 3840, vw, rsw, sw + (32 * (i >> 2) + 16 * ((i >> 1) & 1) + 4 * (i & 1)) * 3840);
      } else {
        unsigned sw = kt * (64 * 3072);
#pragma unroll
        for (int i = 2; i < 6; ++i) dma(sb + lw * 1024 + i * 4096, vw, rsw, sw + (i / 3) * 32 * 3072 + (i % 3) * 128);
      }
    };
    STAMP(10);
    issue1a(0); issue1b(0); STAMP(12); issue1a(1);
    auto rs2 = __builtin_amdgcn_make_buffer_rsrc((void*)w2, 0, 1536 * 1280, 0x00020000);
    auto rso = __builtin_amdgcn_make_buffer_rsrc((void*)wo, 0, 640 * 1280, 0x00020000);
    const unsigned v2h = (16 * tt + 8 * hh + rho) * 1280 + ct * 128 + swz;
    const unsigned v2o = (16 * hh + 8 * (rho >> 2) + 4 * tt + (rho & 3)) * 1280 + ct * 128 + swz;
    const unsigned v2o3 = hh ? 0x80000000u : v2o;
    auto issue2 = [&](int G) {
      unsigned sb = ldsb + ((G + 1) % 3) * SLOT + lw * 1024;
      const bool isO = G >= 4;
      auto rs = isO ? rso : rs2;
      unsigned so = isO ? (G - 4) * 320 * 1280 : G * 384 * 1280;
      const unsigned step = isO ? 16 * 1280 : 32 * 1280;
      const unsigned va = isO ? v2o : v2h, vb = isO ? v2o3 : v2h;
#pragma nounroll
      for (int a = 0; a < 4; ++a) {
        dma(sb, va, rs, so);
        dma(sb + 4096, va, rs, so + 32 * 1280);
        dma(sb + 8192, vb, rs, so + 64 * 1280);
        sb += 12288; so += 64 * 1280 + step;
      }
    };
    const int ir = min(lw * 36 + min(lane, 35), isattn ? 143 : 127);
    int stok;
    { int j = ir - 16; stok = isattn ? (ir < 16 ? P0 - 16 + 2 * (ir & 7) + (ir >> 3) : P0 + (j & ~31) + 2 * (j & 15) + ((j >> 4) & 1)) : P0 + ir; }
    const u64* statp = (const u64*)(ws + WS_STAT) + max(stok, 0);
    waitv<6>();
    STAMP(8);
#pragma nounroll
    for (int t = 0; t < 10; ++t) {
      bar();
      if (t == 5) STAMP(6);
      if (t == 6) STAMP(9);
      u64 gr;
      if (t == 7) asm volatile("global_load_dwordx2 %0, %1, off sc1" : "=v"(gr) : "v"(statp) : "memory");
      if (t == 0) issue1b(1);
      if (t < 8) {
        issue1a(t + 2); issue1b(t + 2);
        if (ext) waitv<13>(); else if (isattn) waitv<12>(); else waitv<10>();
      } else {
        issue2(t - 8);
        waitv<12>();
      }
      if (t == 7) {
        asm volatile("" : "+v"(gr));
        const unsigned tg = *(LAS unsigned*)(lds + AUX_TAG);
#pragma nounroll
        for (int it = 0; it < (1 << 16) && !__all((unsigned)(gr >> 32) == tg); ++it) { __builtin_amdgcn_s_sleep(2); gr = ld_sc1(statp); }
        float r = __builtin_amdgcn_rcpf(hi16((unsigned)gr));
        *(LAS float*)(lds + AUX_R + ir * 4) = r;
        *(LAS float*)(lds + AUX_NM + ir * 4) = -lo16((unsigned)gr) * r;
      }
      if (t == 5) STAMP(7);
    }
    STAMP(1);
    bar();
    bar();
    const u64 tag = *(LAS u64*)(lds + AUX_TAG);
    if (isattn) {
      if (lw == 0) poll(myflag, tag);
      bar();
      bar();
      waitv<0>(); bar();
      issue2(2);
    } else {
      bar();
      issue2(2);
      bar();
      waitv<12>(); bar();
    }
    STAMP(2);
    issue2(3);
    waitv<12>(); bar(); issue2(4);
    waitv<12>(); bar(); issue2(5);
    waitv<12>(); bar();
    waitv<0>(); bar();
    if (b == 0 && lw == 1) {
      const u64* f = flags + (lane >> 4) * 32 + (lane & 8) * 2 + (lane & 7);
#pragma nounroll
      for (int i = 0; i < 4; ++i) poll(f + i * 128, tag);
      if (lane == 0) st_sc1((u64*)ws, tag);
    }
    STAMP(3);
    return;
  }

  char* actp = ws + WS_ACT;
  const bf8 one = ones8();
  u64 tag;
  {
    const int row = P0 + 8 * (grp + (isattn ? 0 : 8)) + w;
    auto rs = __builtin_amdgcn_make_buffer_rsrc((void*)(x + (size_t)row * 640), 0, 1280, 0x00020000);
    bf8 va = __builtin_bit_cast(bf8, __builtin_amdgcn_raw_buffer_load_b128(rs, lane * 16, 0, 0));
    bf8 vb = __builtin_bit_cast(bf8, __builtin_amdgcn_raw_buffer_load_b128(rs, lane * 16 + 1024, 0, 0));
    tag = ld_sc1((const u64*)ws) + 1;
    if (w == 1) *(LAS unsigned*)(lds + AUX_ONES + lane * 4) = 0x3f803f80u;
    float s1 = dot8(vb, one, dot8(va, one, 0.f)), s2 = dot8(vb, vb, dot8(va, va, 0.f));
    float v = red4(s1, s2, 0.f, 0.f);
    v = dpp_add<0xB1>(v); v = dpp_add<0x4E>(v); v = dpp_add<0x141>(v); v = dpp_add<0x140>(v);
    float mean = asf(__builtin_amdgcn_readlane(asu(v), 0)) * (1.f / 640.f);
    float var = fmaxf(asf(__builtin_amdgcn_readlane(asu(v), 16)) * (1.f / 640.f) - mean * mean, 0.f);
    unsigned pkd = pk2(mean, __builtin_amdgcn_sqrtf(bfr(bfr(var) + 1e-5f)));
    if (lane == 0) st_sc1((u64*)(ws + WS_STAT) + row, (tag << 32) | pkd);
    if (w == 0) { *(LAS u64*)(lds + AUX_TAG) = tag; *(LAS unsigned*)(lds + AUX_CNT) = 0; }
  }
  auto rsa = __builtin_amdgcn_make_buffer_rsrc(actp + (size_t)(tb0 + w) * ACT_TB, 0, w < RB ? ACT_TB : 0, 0x00020000);
  u4 R[12];

  if (isattn) {
    const int tau = w >> 2, h = w & 3;
    const int fsw = (c16 >> 1) & 7;
    const int hrow = c16 ^ (tau ? 0 : 8);
    const int xb = (XOFF_A + (16 + 16 * tau + c16) * 128 + ((g ^ fsw) << 4)) ^ (tau << 6);
    int hb = (XOFF_A + hrow * 128 + ((g ^ ((hrow >> 1) & 7)) << 4)) ^ (tau << 6);
    const int hm = c16 != 0;
    if (c16 == 0) hb = AUX_ONES + (hb & 255);
    const int wrow = (16 * (g >> 1) + 4 * (g & 1) + q4) * 480;
    const int wm = wrow + 40 * h + 8 * p4, wr = wrow + 40 * p4 + 32;
    int sg[3];
#pragma unroll
    for (int j = 0; j < 3; ++j) sg[j] = (j + h) % 3;
    f4 Q[4], K[4], V[4], Qh, Kh, Vh, R5[3], X5;
#pragma unroll
    for (int i = 0; i < 4; ++i) { Q[i] = f4{0, 0, 0, 0}; K[i] = Q[i]; V[i] = Q[i]; }
    Qh = Kh = Vh = R5[0] = R5[1] = R5[2] = X5 = Q[0];
    bf8 xf[4], xh, xo, wq, wk, wv, w5[3];
    bar();
    __builtin_amdgcn_s_waitcnt(0xC07F);
    {
      LAS char* px = lds + xb;
      LAS char* pw = lds + tau * 15360 + wm;
      LAS char* pr = lds + tau * 15360 + wr;
#pragma unroll
      for (int j = 0; j < 3; ++j) w5[j] = ldw(pr + sg[j] * 160, 3840);
      xo = ldx(px + h * 4096); SCHED();
      wq = ldw(pw, 3840); SCHED();
      xf[0] = ldx(px); SCHED();
      wk = ldw(pw + 160, 3840); SCHED();
#pragma unroll
      for (int i = 1; i < 4; ++i) { xf[i] = ldx(px + i * 4096); SCHED(); }
      xh = ldx(lds + hb); SCHED();
      wv = ldw(pw + 320, 3840);
    }
    int slot = 0;
#pragma nounroll
    for (int s = 0; s < 10; ++s) {
      const int nslot = slot == 2 * SLOT ? 0 : slot + SLOT;
#pragma unroll
      for (int ks = 0; ks < 2; ++ks) {
        const int ls = ks ? nslot : slot, lk = ks ^ 1;
        LAS char* px = lds + ls + (xb ^ (lk << 6));
        LAS char* pw = lds + ls + (lk ^ tau) * 15360 + wm;
        LAS char* pr = lds + ls + (lk ^ tau) * 15360 + wr;
        LAS char* ph = lds + ((hb + ls * hm) ^ (lk << 6));
        SCHED();
#pragma unroll
        for (int j = 0; j < 3; ++j) R5[j] = mfma(w5[j], xo, R5[j]);
        PIN1(R5[0]); PIN1(R5[1]); PIN1(R5[2]); SCHED();
#pragma unroll
        for (int i = 0; i < 4; ++i) { Q[i] = mfma(wq, xf[i], Q[i]); PIN1(Q[i]); SCHED(); }
        X5 = mfma(w5[0], xh, X5); PIN1(X5);
        if (ks == 0) { Qh = mfma(wq, xh, Qh); PIN1(Qh); }
        SCHED();
        if (ks == 1) bar();
        w5[0] = ldw(pr + sg[0] * 160, 3840); K[0] = mfma(wk, xf[0], K[0]); PIN1(K[0]); SCHED();
        w5[1] = ldw(pr + sg[1] * 160, 3840); K[1] = mfma(wk, xf[1], K[1]); PIN1(K[1]); SCHED();
        w5[2] = ldw(pr + sg[2] * 160, 3840); K[2] = mfma(wk, xf[2], K[2]); PIN1(K[2]); SCHED();
        xo = ldx(px + h * 4096);             K[3] = mfma(wk, xf[3], K[3]); PIN1(K[3]); SCHED();
        wq = ldw(pw, 3840);                  Kh = mfma(wk, xh, Kh); PIN1(Kh); SCHED();
        V[0] = mfma(xf[0], wv, V[0]); PIN1(V[0]); SCHED();
        xf[0] = ldx(px); wk = ldw(pw + 160, 3840);
        V[1] = mfma(xf[1], wv, V[1]); PIN1(V[1]); SCHED();
        xf[1] = ldx(px + 4096);
        V[2] = mfma(xf[2], wv, V[2]); PIN1(V[2]); SCHED();
        xf[2] = ldx(px + 8192);
        V[3] = mfma(xf[3], wv, V[3]); PIN1(V[3]); SCHED();
        xf[3] = ldx(px + 12288);
        Vh = mfma(xh, wv, Vh); PIN1(Vh); SCHED();
        xh = ldx(ph); wv = ldw(pw + 320, 3840);
        SCHED();
      }
      slot = nslot;
    }
    LAS char* cb = lds + AUX_CS;
    if (c16 == 0) {
      *(LAS f4*)(cb + tau * 960 + (20 * h + 4 * g) * 4) = Qh;
      *(LAS f4*)(cb + sg[0] * 320 + (20 * g + 16) * 4) = X5;
    }
    float nm[4], rr[4], nmh, rh, nmo, ro;
    f4 nm4[4], r4[4], nmh4, rh4;
    {
      LAS char* sl = lds + AUX_NM + (16 + 16 * tau + c16) * 4;
      nmo = *(LAS float*)(sl + h * 128); ro = *(LAS float*)(sl + 576 + h * 128);
      nmh = *(LAS float*)(lds + AUX_NM + hrow * 4); rh = *(LAS float*)(lds + AUX_R + hrow * 4);
#pragma unroll
      for (int i = 0; i < 4; ++i) { nm[i] = *(LAS float*)(sl + i * 128); rr[i] = *(LAS float*)(sl + 576 + i * 128); }
    }
    f4 csk;
#pragma unroll
    for (int j = 0; j < 4; ++j) csk[j] = asf(__builtin_amdgcn_update_dpp(0u, asu(Kh[j]), 0x150, 0xf, 0xf, true));
    const float csv = asf(__builtin_amdgcn_ds_bpermute(c16 * 4, asu(Vh[0])));
    STAMP(1);
    bar();
    u2 Qp[4], Kp[5], Vp[5];
    LAS char* rb = lds + SC_REM + tau * 7680 + c16 * 32 + g * 8;
    {
      f4 c5[3];
#pragma unroll
      for (int j = 0; j < 3; ++j) c5[j] = *(LAS f4*)(cb + sg[j] * 320 + (20 * g + 16) * 4);
      f4 csq = *(LAS f4*)(cb + (20 * h + 4 * g) * 4), csq2 = *(LAS f4*)(cb + 960 + (20 * h + 4 * g) * 4);
      SCHED();
      Kp[0] = pk4(Kh * rh + csk * nmh);
#pragma unroll
      for (int i = 0; i < 4; ++i) Kp[i + 1] = pk4(K[i] * rr[i] + csk * nm[i]);
      PIN4(Kp[1], Kp[2], Kp[3], Kp[4]); PIN1(Kp[0]);
      SCHED();
#pragma unroll
      for (int j = 0; j < 3; ++j) *(LAS u2*)(rb + sg[j] * 2560 + (h + 1) * 512) = pk4(R5[j] * ro + c5[j] * nmo);
      *(LAS u2*)(rb + sg[0] * 2560) = pk4(X5 * rh + c5[0] * nmh);
      SCHED();
      LAS char* sr = lds + AUX_NM + (16 + 16 * tau + 4 * g) * 4;
      const int h4 = (4 * g) ^ (tau ? 0 : 8);
      nmh4 = *(LAS f4*)(lds + AUX_NM + h4 * 4); rh4 = *(LAS f4*)(lds + AUX_R + h4 * 4);
#pragma unroll
      for (int i = 0; i < 4; ++i) { nm4[i] = *(LAS f4*)(sr + i * 128); r4[i] = *(LAS f4*)(sr + 576 + i * 128); }
      SCHED();
      csq += csq2;
#pragma unroll
      for (int i = 0; i < 4; ++i) Qp[i] = pk4(Q[i] * rr[i] + csq * nm[i]);
      PIN4(Qp[0], Qp[1], Qp[2], Qp[3]);
    }
    STAMP(4);
    bar();
    {
      LAS char* rt = lds + SC_REM + tau * 7680 + 5120 + lane * 8;
      u2 K5p[5], V5p[5], q5[4];
#pragma unroll
      for (int i = 0; i < 5; ++i) {
        K5p[i] = *(LAS u2*)(rb + 2560 + i * 512);
        if (i < 4) q5[i] = *(LAS u2*)(rb + (i + 1) * 512);
      }
#pragma unroll
      for (int i = 0; i < 5; ++i) V5p[i] = __builtin_bit_cast(u2, __builtin_amdgcn_ds_read_tr16_b64_v4bf16((LAS bf4*)(rt + i * 512)));
      SCHED();
      Vp[0] = pk4(Vh * rh4 + nmh4 * csv);
#pragma unroll
      for (int i = 0; i < 4; ++i) Vp[i + 1] = pk4(V[i] * r4[i] + nm4[i] * csv);
      PIN4(Vp[1], Vp[2], Vp[3], Vp[4]); PIN1(Vp[0]);
      f4 mcur, mprev; u2 bcur;
#pragma unroll
      for (int j = 0; j < 4; ++j) {
        int d = c16 - 4 * g - j;
        mcur[j] = (unsigned)d < 8u ? 0.f : -1e30f;
        mprev[j] = d < -8 ? 0.f : -1e30f;
      }
#pragma unroll
      for (int j = 0; j < 2; ++j) {
        int key = 4 * g + 2 * j;
        bcur[j] = (key <= c16 ? 0xffffu : 0u) | (key + 1 <= c16 ? 0xffff0000u : 0u);
      }
      SCHED();
#pragma unroll
      for (int ks = 0; ks < 12; ++ks) R[ks] = __builtin_amdgcn_raw_buffer_load_b128(rsa, lane * 16, ks * 1024, 16);
      SCHED();
      const float cexp = 0.22360679774997896f * 1.4426950408889634f;
      f4 sc[4], sp[4];
#pragma unroll
      for (int i = 0; i < 4; ++i) {
        if (g != h) q5[i] = u2{0, 0};
        bf8 qb = cat(Qp[i], q5[i]);
        f4 mp = mprev;
        if (i == 0 && panel == 0) mp = f4{-1e30f, -1e30f, -1e30f, -1e30f};
        sc[i] = mfma(cat(Kp[i + 1], K5p[i + 1]), qb, mcur);
        sp[i] = mfma(cat(Kp[i], K5p[i]), qb, mp);
      }
      bf8 pb[4];
#pragma unroll
      for (int i = 0; i < 4; ++i) {
        f4 sv;
#pragma unroll
        for (int j = 0; j < 4; ++j) sv[j] = fmx(sc[i][j], sp[i][j]);
        float mc = xg_max(fmx(fmx(sv[0], sv[1]), fmx(sv[2], sv[3]))) * cexp;
        f4 pv;
#pragma unroll
        for (int j = 0; j < 4; ++j) pv[j] = __builtin_amdgcn_exp2f(sv[j] * cexp - mc);
        u2 pp = pk4(pv);
        pb[i] = cat(pp & bcur, pp & ~bcur);
      }
      LAS char* ob = lds + SC_O + (2 * c16 + tau) * OPITCH + 40 * h;
      LAS char* ob5 = g == h ? ob + 32 : lds + SC_O + 160;
      const f4 zero = {0, 0, 0, 0};
#pragma unroll
      for (int i = 0; i < 4; ++i) {
        f4 l = mfma(one, pb[i], zero);
        f4 o = mfma(cat(Vp[i + 1], Vp[i]), pb[i], zero);
        f4 o5 = mfma(cat(V5p[i + 1], V5p[i]), pb[i], zero);
        float inv = __builtin_amdgcn_rcpf(l[0]);
        *(LAS u2*)(ob + i * 32 * OPITCH + 8 * g) = pk4(o * inv);
        *(LAS u2*)(ob5 + i * 32 * OPITCH) = pk4(o5 * inv);
      }
    }
    STAMP(5);
    bar();
    if (w >= 4) {
#pragma unroll
      for (int i = 0; i < 2; ++i) {
        const int tl = 2 * (w - 4) + i;
        LAS char* op = lds + SC_O + (16 * tl + c16) * OPITCH + 16 * g;
        u4 f0 = *(LAS u4*)op, f1 = *(LAS u4*)(op + 64), f2v = *(LAS u4*)(op + (g < 2 ? 128 : 0));
        auto rs = __builtin_amdgcn_make_buffer_rsrc(actp + (size_t)(panel * 8 + tl) * ACT_TB + (48 + 3 * grp) * 1024, 0, 2560, 0x00020000);
        __builtin_amdgcn_raw_buffer_store_b128(f0, rs, lane * 16, 0, 16);
        __builtin_amdgcn_raw_buffer_store_b128(f1, rs, lane * 16, 1024, 16);
        __builtin_amdgcn_raw_buffer_store_b128(f2v, rs, lane * 16 + 2048, 0, 16);
      }
      waitv<0>();
      if (lane == 0 && __hip_atomic_fetch_add((LAS unsigned*)(lds + AUX_CNT), 1u, __ATOMIC_RELAXED, __HIP_MEMORY_SCOPE_WORKGROUP) == 3)
        st_sc1(flags + panel * 32 + 16 + grp, tag);
      STAMP(2);
    }
  } else {
    const int tq = w & 3, fh = w >> 2;
    const int rho = 4 * (g & 1) + q4;
    const int role = (tq + 2 * fh) & 3;
    const int xb = XOFF_F + (32 * tq + c16) * 128 + ((g ^ ((c16 >> 1) & 7)) << 4);
    const int wl = (g >> 1) * 2048 + rho * 128 + (((p4 >> 1) ^ (rho & 6)) << 4) + (p4 & 1) * 8;
    int wb[3], pu[3];
    u2 bias[6];
#pragma unroll
    for (int u = 0; u < 3; ++u) {
      pu[u] = 3 * fh + (u + role) % 3;
      wb[u] = (wl ^ ((pu[u] & 1) << 6)) + (pu[u] >> 1) * 4096;
      bias[2 * u] = *(const u2*)(b1 + grp * 192 + pu[u] * 32 + 4 * g);
      bias[2 * u + 1] = *(const u2*)(b1 + grp * 192 + pu[u] * 32 + 16 + 4 * g);
    }
    f4 acc[2][6], CS[2];
#pragma unroll
    for (int e = 0; e < 6; ++e) { acc[0][e] = f4{0, 0, 0, 0}; acc[1][e] = acc[0][e]; }
    CS[0] = CS[1] = acc[0][0];
    bf8 onev = one;
    asm volatile("" : "+v"(onev));
    bf8 xf[2][2], wf[6];
    bar();
    __builtin_amdgcn_s_waitcnt(0xC07F);
    xf[0][0] = ldx(lds + xb); xf[0][1] = ldx(lds + xb + 2048);
#pragma unroll
    for (int e = 0; e < 6; ++e) wf[e] = ldw(lds + (wb[e >> 1] ^ ((e & 1) << 5)), 1024);
    int slot = 0;
#pragma nounroll
    for (int s = 0; s < 10; ++s) {
      const int nslot = slot == 2 * SLOT ? 0 : slot + SLOT;
#pragma unroll
      for (int ks = 0; ks < 2; ++ks) {
        const int lk = ks ^ 1;
        LAS char* sp = lds + (ks ? nslot : slot);
        SCHED();
#pragma unroll
        for (int e = 0; e < 2; ++e) {
          acc[0][e] = mfma(wf[e], xf[ks][0], acc[0][e]); acc[1][e] = mfma(wf[e], xf[ks][1], acc[1][e]);
          PIN1(acc[0][e]); PIN1(acc[1][e]); SCHED();
        }
        asm volatile("s_cmp_eq_u32 %5, 3\n\ts_cbranch_scc1 .Lcs%=\n\tv_mfma_f32_16x16x32_bf16 %0, %2, %4, %0\n\tv_mfma_f32_16x16x32_bf16 %1, %3, %4, %1\n.Lcs%=:"
                     : "+v"(CS[0]), "+v"(CS[1]) : "v"(wf[0]), "v"(wf[1]), "v"(onev), "s"(role) : "scc");
        SCHED();
        if (ks == 1) bar();
#pragma unroll
        for (int e = 2; e < 6; ++e) {
          if (e == 2) { xf[lk][0] = ldx(sp + (xb ^ (lk << 6))); xf[lk][1] = ldx(sp + (xb ^ (lk << 6)) + 2048); }
          else wf[e - 1] = ldw(sp + (wb[(e - 1) >> 1] ^ (((e - 1) & 1) << 5)) + lk * 12288, 1024);
          acc[0][e] = mfma(wf[e], xf[ks][0], acc[0][e]); PIN1(acc[0][e]); SCHED();
          if (e < 4) wf[e - 2] = ldw(sp + (wb[0] ^ ((e & 1) << 5)) + lk * 12288, 1024);
          acc[1][e] = mfma(wf[e], xf[ks][1], acc[1][e]); PIN1(acc[1][e]); SCHED();
        }
        wf[5] = ldw(sp + (wb[2] ^ 32) + lk * 12288, 1024);
        SCHED();
      }
      slot = nslot;
    }
    asm volatile("s_nop 7" : "+v"(CS[0]), "+v"(CS[1]));
    if (role < 3 && c16 < 2) *(LAS f4*)(lds + AUX_CS + (pu[0] * 32 + c16 * 16 + 4 * g) * 4) = c16 ? CS[1] : CS[0];
    float nm[2], rr[2];
#pragma unroll
    for (int i = 0; i < 2; ++i) { nm[i] = *(LAS float*)(lds + AUX_NM + (32 * tq + 16 * i + c16) * 4); rr[i] = *(LAS float*)(lds + AUX_R + (32 * tq + 16 * i + c16) * 4); }
    STAMP(1);
    bar();
    f4 cb4[6];
#pragma unroll
    for (int e = 0; e < 6; ++e) cb4[e] = *(LAS f4*)(lds + AUX_CS + (pu[e >> 1] * 32 + (e & 1) * 16 + 4 * g) * 4);
#pragma unroll
    for (int i = 0; i < 2; ++i) {
      const int tl = 2 * tq + i;
      auto rs = __builtin_amdgcn_make_buffer_rsrc(actp + (size_t)(panel * 8 + tl) * ACT_TB + 6 * grp * 1024, 0, 6144, 0x00020000);
#pragma unroll
      for (int u = 0; u < 3; ++u) {
        u2 hp[2];
#pragma unroll
        for (int d = 0; d < 2; ++d) {
          const int e = 2 * u + d;
          f4 bb = {lo16(bias[e][0]), hi16(bias[e][0]), lo16(bias[e][1]), hi16(bias[e][1])};
          f4 v = acc[i][e] * rr[i] + (cb4[e] * nm[i] + bb);
#pragma unroll
          for (int j = 0; j < 4; ++j) v[j] = v[j] * fmx(v[j], 0.f);
          hp[d] = pk4(v);
        }
        __builtin_amdgcn_raw_buffer_store_b128(u4{hp[0][0], hp[0][1], hp[1][0], hp[1][1]}, rs, lane * 16, pu[u] * 1024, 16);
      }
    }
    STAMP(6);
    bar();
    waitv<0>();
    if (lane == 0 && __hip_atomic_fetch_add((LAS unsigned*)(lds + AUX_CNT), 1u, __ATOMIC_RELAXED, __HIP_MEMORY_SCOPE_WORKGROUP) == 7)
      st_sc1(flags + panel * 32 + grp, tag);
    STAMP(2);
    if (w == 7) poll(myflag, tag);
    bar();
#pragma unroll
    for (int ks = 0; ks < 12; ++ks) R[ks] = __builtin_amdgcn_raw_buffer_load_b128(rsa, lane * 16, ks * 1024, 16);
  }

  if (w >= RB) {
    bar(); bar();
    if (w == 7) poll(myflag + 16, tag);
    bar(); bar(); bar();
    return;
  }
  {
    STAMP(7);
    const int tb = tb0 + w;
    const int rho = 4 * (g & 1) + q4;
    const int wl = (g >> 1) * 2048 + rho * 128 + (((p4 >> 1) ^ (rho & 6)) << 4) + (p4 & 1) * 8;
    u2 bias[4];
#pragma unroll
    for (int c = 0; c < 4; ++c) bias[c] = *(const u2*)(b2 + ct * 64 + c * 16 + 4 * g);
    const size_t xo = (size_t)(tb * 16 + c16) * 640 + ct * 64 + g * 16;
    const u4 xr0 = *(const u4*)(x + xo), xr1 = *(const u4*)(x + xo + 8);
    f4 acc[4], gate[4];
#pragma unroll
    for (int c = 0; c < 4; ++c) { acc[c] = f4{0, 0, 0, 0}; gate[c] = acc[c]; }
    unsigned va = lane * 16, va3 = va;
    int slot = SLOT;
#pragma nounroll
    for (int G = 0; G < 6; ++G) {
      if (G > 0) bar();
      if (G == 1) STAMP(8);
      if (G == 2) STAMP(9);
      if (G == 3) STAMP(10);
      if (G == 4) STAMP(11);
      if (G == 5) STAMP(12);
      va += 12288;
      va3 = (G >= 3 && lane >= 32) ? 0x80000000u : va;
      LAS char* sp = lds + slot;
      bf8 wf[2][4];
#pragma unroll
      for (int c = 0; c < 4; ++c) wf[0][c] = ldw(sp + (wl ^ (c << 5)), 1024);
      SCHED();
#pragma unroll
      for (int ks = 0; ks < 12; ++ks) {
        bf8 a = __builtin_bit_cast(bf8, R[ks]);
#pragma unroll
        for (int c = 0; c < 4; ++c) {
          if (ks < 11) wf[(ks + 1) & 1][c] = ldw(sp + (wl ^ (c << 5)) + (ks + 1) * 4096, 1024);
          acc[c] = mfma(wf[ks & 1][c], a, acc[c]);
          asm volatile("" : "+v"(acc[c]));
          SCHED();
        }
        R[ks] = __builtin_amdgcn_raw_buffer_load_b128(rsa, (ks % 3 == 2) ? va3 : va, ks * 1024, 16);
        SCHED();
      }
      if (G == 3) {
#pragma unroll
        for (int c = 0; c < 4; ++c) {
          f4 bb = {lo16(bias[c][0]), hi16(bias[c][0]), lo16(bias[c][1]), hi16(bias[c][1])};
          f4 f = acc[c] + bb;
#pragma unroll
          for (int j = 0; j < 4; ++j) gate[c][j] = f[j] * __builtin_amdgcn_rcpf(1.f + __builtin_amdgcn_exp2f(f[j] * -1.4426950408889634f));
          acc[c] = f4{0, 0, 0, 0};
        }
      }
      slot = slot == 2 * SLOT ? 0 : slot + SLOT;
    }
    u2 Y[4];
#pragma unroll
    for (int c = 0; c < 4; ++c) Y[c] = pk4(acc[c] * gate[c]);
#pragma unroll
    for (int d = 0; d < 2; ++d) {
      auto s0 = __builtin_amdgcn_permlane32_swap(Y[0][d], Y[2][d], false, false);
      auto s1 = __builtin_amdgcn_permlane32_swap(Y[1][d], Y[3][d], false, false);
      auto t0 = __builtin_amdgcn_permlane16_swap(s0[0], s1[0], false, false);
      auto t1 = __builtin_amdgcn_permlane16_swap(s0[1], s1[1], false, false);
      Y[0][d] = t0[0]; Y[1][d] = t0[1]; Y[2][d] = t1[0]; Y[3][d] = t1[1];
    }
    u4 o0, o1;
#pragma unroll
    for (int d = 0; d < 4; ++d) {
      unsigned y0 = Y[d >> 1][d & 1], y1 = Y[2 + (d >> 1)][d & 1];
      o0[d] = pk2(lo16(xr0[d]) + lo16(y0), hi16(xr0[d]) + hi16(y0));
      o1[d] = pk2(lo16(xr1[d]) + lo16(y1), hi16(xr1[d]) + hi16(y1));
    }
    *(u4*)(out + xo) = o0;
    *(u4*)(out + xo + 8) = o1;
    STAMP(3);
  }
}

extern "C" void kernel_launch(void* const* d_in, const int* in_sizes, int n_in, void* d_out, int out_size, void* d_ws, size_t ws_size,
                              hipStream_t stream) {
  (void)in_sizes; (void)n_in; (void)out_size; (void)ws_size;
  (void)hipFuncSetAttribute((const void*)fused, hipFuncAttributeMaxDynamicSharedMemorySize, LDS_BYTES);
  fused<<<256, 768, LDS_BYTES, stream>>>((const u16*)d_in[0], (const u16*)d_in[1], (const u16*)d_in[2], (const u16*)d_in[4],
                                         (const u16*)d_in[5], (const u16*)d_in[6], (const u16*)d_in[7], (u16*)d_out, (char*)d_ws);
}
```

```cpp
#define STAMPS 1
#include <hip/hip_runtime.h>
#include <stdint.h>

typedef unsigned short u16;
typedef unsigned long long u64;
typedef __bf16 bf8 __attribute__((ext_vector_type(8)));
typedef __bf16 bf4 __attribute__((ext_vector_type(4)));
typedef __bf16 bf2 __attribute__((ext_vector_type(2)));
typedef float f4 __attribute__((ext_vector_type(4)));
typedef float f2 __attribute__((ext_vector_type(2)));
typedef unsigned u4 __attribute__((ext_vector_type(4)));
typedef unsigned u2 __attribute__((ext_vector_type(2)));
#define LAS __attribute__((address_space(3)))
#define GAS __attribute__((address_space(1)))
#define DI __device__ __forceinline__

constexpr int SLOT = 49152;
constexpr int AUX = 3 * SLOT;
constexpr int AUX_ONES = AUX;
constexpr int AUX_NM = AUX + 256;
constexpr int AUX_R = AUX_NM + 576;
constexpr int AUX_CS = AUX_NM + 1152;
constexpr int AUX_TAG = AUX_CS + 1280;
constexpr int AUX_CNT = AUX_TAG + 8;
constexpr int AUX_DUMMY = AUX_TAG + 16;
constexpr int LDS_BYTES = AUX_DUMMY + 16;
constexpr int XOFF_A = 30720;
constexpr int XOFF_F = 24576;
constexpr int SC_REM = 0;
constexpr int SC_O = 16384;
constexpr int OPITCH = 176;

constexpr size_t WS_FLAG = 4096;
constexpr size_t WS_STAT = 16384;
constexpr size_t WS_STAMP = 65536;
constexpr size_t WS_ACT = 1 << 20;
constexpr int ACT_TB = 72 * 1024;

constexpr int RBF = 6, RBA = 4;

DI void bar() { asm volatile("s_waitcnt lgkmcnt(0)\n\ts_barrier" ::: "memory"); }
template <int N> DI void waitv() { asm volatile("s_waitcnt vmcnt(%0)" ::"i"(N) : "memory"); }
DI f4 mfma(bf8 a, bf8 b, f4 c) { return __builtin_amdgcn_mfma_f32_16x16x32_bf16(a, b, c, 0, 0, 0); }
DI unsigned rfl(unsigned v) { return __builtin_amdgcn_readfirstlane(v); }
DI void dma(unsigned ldsaddr, unsigned voff, __amdgpu_buffer_rsrc_t rs, unsigned soff) {
  unsigned keep;
  asm volatile("s_mov_b32 %0, m0\n\ts_mov_b32 m0, %1\n\ts_nop 0\n\tbuffer_load_dwordx4 %2, %3, %4 offen lds\n\ts_mov_b32 m0, %0"
               : "=&s"(keep) : "s"(ldsaddr), "v"(voff), "s"(rs), "s"(soff) : "memory");
}
DI void dma60(unsigned ldsaddr, unsigned voff, __amdgpu_buffer_rsrc_t rs, unsigned soff) {
  unsigned keep;
  asm volatile("s_mov_b32 %0, m0\n\ts_mov_b32 m0, %1\n\ts_mov_b32 exec_hi, 0x0fffffff\n\tbuffer_load_dwordx4 %2, %3, %4 offen lds\n\ts_mov_b32 exec_hi, -1\n\ts_mov_b32 m0, %0"
               : "=&s"(keep) : "s"(ldsaddr), "v"(voff), "s"(rs), "s"(soff) : "memory");
}
DI void mfma_ip(f4& c, bf8 a, bf8 b) { asm volatile("v_mfma_f32_16x16x32_bf16 %0, %1, %2, %0" : "+v"(c) : "v"(a), "v"(b)); }
DI bf8 ldx(LAS char* p) { return *(LAS bf8*)p; }
DI bf8 ldw(LAS char* p, int tstride) {
  bf4 a = __builtin_amdgcn_ds_read_tr16_b64_v4bf16((LAS bf4*)p);
  bf4 b = __builtin_amdgcn_ds_read_tr16_b64_v4bf16((LAS bf4*)(p + tstride));
  return __builtin_shufflevector(a, b, 0, 1, 2, 3, 4, 5, 6, 7);
}
DI float dot8(bf8 a, bf8 b, float c) {
  c = __builtin_amdgcn_fdot2_f32_bf16(__builtin_shufflevector(a, a, 0, 1), __builtin_shufflevector(b, b, 0, 1), c, false);
  c = __builtin_amdgcn_fdot2_f32_bf16(__builtin_shufflevector(a, a, 2, 3), __builtin_shufflevector(b, b, 2, 3), c, false);
  c = __builtin_amdgcn_fdot2_f32_bf16(__builtin_shufflevector(a, a, 4, 5), __builtin_shufflevector(b, b, 4, 5), c, false);
  c = __builtin_amdgcn_fdot2_f32_bf16(__builtin_shufflevector(a, a, 6, 7), __builtin_shufflevector(b, b, 6, 7), c, false);
  return c;
}
DI bf8 ones8() { u4 o = {0x3f803f80u, 0x3f803f80u, 0x3f803f80u, 0x3f803f80u}; return __builtin_bit_cast(bf8, o); }
DI unsigned pk2(float a, float b) { f2 v = {a, b}; return __builtin_bit_cast(unsigned, __builtin_convertvector(v, bf2)); }
DI u2 pk4(f4 v) { u2 r = {pk2(v[0], v[1]), pk2(v[2], v[3])}; return r; }
DI float bfr(float a) { return __builtin_bit_cast(float, pk2(a, 0.f) << 16); }
DI float lo16(unsigned u) { return __builtin_bit_cast(float, u << 16); }
DI float hi16(unsigned u) { return __builtin_bit_cast(float, u & 0xffff0000u); }
DI bf8 cat(u2 a, u2 b) { u4 r = {a[0], a[1], b[0], b[1]}; return __builtin_bit_cast(bf8, r); }
DI float asf(unsigned u) { return __builtin_bit_cast(float, u); }
DI unsigned asu(float f) { return __builtin_bit_cast(unsigned, f); }
DI float xg_max(float v) {
  auto a = __builtin_amdgcn_permlane16_swap(asu(v), asu(v), false, false);
  v = __builtin_elementwise_maximum(asf(a[0]), asf(a[1]));
  auto b = __builtin_amdgcn_permlane32_swap(asu(v), asu(v), false, false);
  return __builtin_elementwise_maximum(asf(b[0]), asf(b[1]));
}
DI float xg_sum(float v) {
  auto a = __builtin_amdgcn_permlane16_swap(asu(v), asu(v), false, false);
  v = asf(a[0]) + asf(a[1]);
  auto b = __builtin_amdgcn_permlane32_swap(asu(v), asu(v), false, false);
  return asf(b[0]) + asf(b[1]);
}
DI float red4(float a, float b, float c, float d) {
  auto u = __builtin_amdgcn_permlane16_swap(asu(a), asu(b), false, false);
  auto v = __builtin_amdgcn_permlane16_swap(asu(c), asu(d), false, false);
  float ab = asf(u[0]) + asf(u[1]), cd = asf(v[0]) + asf(v[1]);
  auto t = __builtin_amdgcn_permlane32_swap(asu(ab), asu(cd), false, false);
  return asf(t[0]) + asf(t[1]);
}
template <int CTRL> DI float dpp_add(float v) { return v + asf(__builtin_amdgcn_update_dpp(0u, asu(v), CTRL, 0xf, 0xf, true)); }
DI float fmx(float a, float b) { return __builtin_elementwise_maximum(a, b); }
#define PIN4(a, b, c, d) asm volatile("" : "+v"(a), "+v"(b), "+v"(c), "+v"(d))
#define PIN1(a) asm volatile("" : "+v"(a))
#define SCHED() __builtin_amdgcn_sched_barrier(0)
#define SGB(mask, n) __builtin_amdgcn_sched_group_barrier(mask, n, 0)
DI u64 ld_sc1(const u64* p) { return __hip_atomic_load((GAS u64*)p, __ATOMIC_RELAXED, __HIP_MEMORY_SCOPE_AGENT); }
DI void st_sc1(u64* p, u64 v) { __hip_atomic_store((GAS u64*)p, v, __ATOMIC_RELAXED, __HIP_MEMORY_SCOPE_AGENT); }
DI void poll(const u64* p, u64 tag) {
#pragma nounroll
  for (int it = 0; it < (1 << 16); ++it) {
    u64 v = ld_sc1(p);
    if (__all(v == tag)) break;
    __builtin_amdgcn_s_sleep(2);
  }
  __builtin_amdgcn_fence(__ATOMIC_ACQUIRE, "wavefront");
}
#define STAMP(i) do { if (lane == 0) ((u64*)(ws + WS_STAMP))[(blockIdx.x * 12 + w) * 16 + (i)] = (STAMPS == 2 ? __builtin_amdgcn_s_memtime() : __builtin_amdgcn_s_memrealtime()); } while (0)
#define LSTAMP(i)

__global__ __launch_bounds__(768, 3) void fused(const u16* __restrict__ x, const u16* __restrict__ wqkv, const u16* __restrict__ wo,
                                                const u16* __restrict__ w1, const u16* __restrict__ b1, const u16* __restrict__ w2,
                                                const u16* __restrict__ b2, u16* __restrict__ out, char* __restrict__ ws) {
  extern __shared__ __attribute__((aligned(16))) char smem[];
  LAS char* lds = (LAS char*)smem;
  const unsigned ldsb = (unsigned)(size_t)lds;
  const int tid = threadIdx.x, lane = tid & 63, w = rfl(tid >> 6);
  const int b = blockIdx.x, xcd = b & 7, loc = b >> 3;
  const int rq = xcd >> 1, half = xcd & 1;
  const bool isattn = loc < 16;
  const int li = loc & 15;
  const int panel = 4 * rq + (li & 3);
  const int grp = 4 * half + (li >> 2);
  const int P0 = panel * 128;
#define ROT 0
  const int rot = ROT;
  int ctl, rb0;
  if (isattn) { if (li < 6) { ctl = li >> 1; rb0 = 24 + 4 * (li & 1); } else { ctl = 3 + (li - 6) / 5; rb0 = 12 + 4 * ((li - 6) % 5); } }
  else        { if (li < 12) { ctl = li >> 2; rb0 = 6 * (li & 3); } else { ctl = 3 + ((li - 12) >> 1); rb0 = 6 * (li & 1); } }
  const int ct = 5 * half + ctl;
  const int tb0 = rq * 32 + rb0;
  const int RB = isattn ? RBA : RBF;
  const int g = lane >> 4, c16 = lane & 15, q4 = (lane >> 2) & 3, p4 = lane & 3;
  u64* flags = (u64*)(ws + WS_FLAG);
  const u64* myflag = flags + ((lane & 8) ? (tb0 + RB - 1) >> 3 : tb0 >> 3) * 32 + (lane & 7);
  STAMP(0);

  if (w >= 8) {
    __builtin_amdgcn_s_setprio(3);
    const int lw = w - 8;
    const int rho = lane >> 3, cs8 = lane & 7, hh = lw >> 1, tt = lw & 1;
    auto rsx = __builtin_amdgcn_make_buffer_rsrc((void*)x, 0, 2048 * 1280, 0x00020000);
    auto rsw = isattn ? __builtin_amdgcn_make_buffer_rsrc((void*)wqkv, 0, 640 * 3840, 0x00020000)
                      : __builtin_amdgcn_make_buffer_rsrc((void*)w1, 0, 640 * 3072, 0x00020000);
    const unsigned swz = (cs8 ^ (rho & 6)) << 4;
    const unsigned vx = rho * (isattn ? 2560 : 1280) + ((cs8 ^ (rho >> 1) ^ (tt << 2)) << 4);
    unsigned vw;
    if (isattn) {
      int r2 = lane >= 30, cc = lane - 30 * r2, seg = (cc >= 10) + (cc >= 20);
      vw = (8 * hh + 2 * tt + r2) * 3840 + seg * 1120 + cc * 16 + grp * 160;
    } else {
      vw = (16 * hh + 4 * tt + (rho >> 2) * 8 + (rho & 3)) * 3072 + grp * 384 + swz;
    }
    unsigned xrow[5];
#pragma unroll
    for (int i = 0; i < 5; ++i) {
      int n = lw + 4 * i, m = n - 2;
      int ta = n < 2 ? P0 - 16 + n : P0 + 32 * (m >> 2) + 16 * (m & 1) + ((m >> 1) & 1);
      ta = ta < 0 ? 0 : ta;
      xrow[i] = (isattn ? ta : P0 + 8 * n) * 1280;
    }
    const bool ext = isattn && lw < 2;
    asm volatile("s_nop 4" ::: "memory");

    auto issue1a = [&](int t) {
      int kt = t + rot; kt = kt >= 10 ? kt - 10 : kt;
      unsigned sb = ldsb + (t % 3) * SLOT;
      unsigned sx = kt * 128;
      unsigned xb = sb + (isattn ? XOFF_A : XOFF_F) + lw * 1024;
#pragma unroll
      for (int i = 0; i < 4; ++i) dma(xb + i * 4096, vx, rsx, sx + xrow[i]);
      if (isattn) {
        unsigned sw = kt * (64 * 3840);
#pragma unroll
        for (int i = 0; i < 2; ++i) dma60(sb + lw * 960 + i * 3840, vw, rsw, sw + (32 * (i >> 2) + 16 * ((i >> 1) & 1) + 4 * (i & 1)) * 3840);
      } else {
        unsigned sw = kt * (64 * 3072);
#pragma unroll
        for (int i = 0; i < 2; ++i) dma(sb + lw * 1024 + i * 4096, vw, rsw, sw + (i / 3) * 32 * 3072 + (i % 3) * 128);
      }
    };
    auto issue1b = [&](int t) {
      int kt = t + rot; kt = kt >= 10 ? kt - 10 : kt;
      unsigned sb = ldsb + (t % 3) * SLOT;
      if (isattn) {
        unsigned sw = kt * (64 * 3840);
        if (ext) dma(sb + XOFF_A + lw * 1024 + 4 * 4096, vx, rsx, kt * 128 + xrow[4]);
#pragma unroll
        for (int i = 2; i < 8; ++i) dma60(sb + lw * 960 + i * 3840, vw, rsw, sw + (32 * (i >> 2) + 16 * ((i >> 1) & 1) + 4 * (i & 1)) * 3840);
      } else {
        unsigned sw = kt * (64 * 3072);
#pragma unroll
        for (int i = 2; i < 6; ++i) dma(sb + lw * 1024 + i * 4096, vw, rsw, sw + (i / 3) * 32 * 3072 + (i % 3) * 128);
      }
    };
    STAMP(10);
    issue1a(0); issue1b(0); STAMP(12); issue1a(1);
    auto rs2 = __builtin_amdgcn_make_buffer_rsrc((void*)w2, 0, 1536 * 1280, 0x00020000);
    auto rso = __builtin_amdgcn_make_buffer_rsrc((void*)wo, 0, 640 * 1280, 0x00020000);
    const unsigned v2h = (16 * tt + 8 * hh + rho) * 1280 + ct * 128 + swz;
    const unsigned v2o = (16 * hh + 8 * (rho >> 2) + 4 * tt + (rho & 3)) * 1280 + ct * 128 + swz;
    const unsigned v2o3 = hh ? 0x80000000u : v2o;
    auto issue2 = [&](int G) {
      unsigned sb = ldsb + ((G + 1) % 3) * SLOT + lw * 1024;
      const bool isO = G >= 4;
      auto rs = isO ? rso : rs2;
      unsigned so = isO ? (G - 4) * 320 * 1280 : G * 384 * 1280;
      const unsigned step = isO ? 16 * 1280 : 32 * 1280;
      const unsigned va = isO ? v2o : v2h, vb = isO ? v2o3 : v2h;
#pragma nounroll
      for (int a = 0; a < 4; ++a) {
        dma(sb, va, rs, so);
        dma(sb + 4096, va, rs, so + 32 * 1280);
        dma(sb + 8192, vb, rs, so + 64 * 1280);
        sb += 12288; so += 64 * 1280 + step;
      }
    };
    const int ir = min(lw * 36 + min(lane, 35), isattn ? 143 : 127);
    int stok;
    { int j = ir - 16; stok = isattn ? (ir < 16 ? P0 - 16 + 2 * (ir & 7) + (ir >> 3) : P0 + (j & ~31) + 2 * (j & 15) + ((j >> 4) & 1)) : P0 + ir; }
    const u64* statp = (const u64*)(ws + WS_STAT) + max(stok, 0);
    waitv<6>();
    STAMP(8);
#pragma nounroll
    for (int t = 0; t < 10; ++t) {
      bar();
      if (t == 5) STAMP(6);
      if (t == 6) STAMP(9);
      u64 gr;
      if (t == 7) asm volatile("global_load_dwordx2 %0, %1, off sc1" : "=v"(gr) : "v"(statp) : "memory");
      if (t == 0) issue1b(1);
      if (t < 8) {
        issue1a(t + 2); issue1b(t + 2);
        if (ext) waitv<13>(); else if (isattn) waitv<12>(); else waitv<10>();
      } else {
        issue2(t - 8);
        waitv<12>();
      }
      if (t == 7) {
        asm volatile("" : "+v"(gr));
        const unsigned tg = *(LAS unsigned*)(lds + AUX_TAG);
#pragma nounroll
        for (int it = 0; it < (1 << 16) && !__all((unsigned)(gr >> 32) == tg); ++it) { __builtin_amdgcn_s_sleep(2); gr = ld_sc1(statp); }
        float r = __builtin_amdgcn_rcpf(hi16((unsigned)gr));
        *(LAS float*)(lds + AUX_R + ir * 4) = r;
        *(LAS float*)(lds + AUX_NM + ir * 4) = -lo16((unsigned)gr) * r;
      }
      if (t == 5) STAMP(7);
    }
    STAMP(1);
    bar();
    bar();
    const u64 tag = *(LAS u64*)(lds + AUX_TAG);
    if (isattn) {
      if (lw == 0) poll(myflag, tag);
      bar();
      bar();
      waitv<0>(); bar();
      issue2(2);
    } else {
      bar();
      issue2(2);
      bar();
      waitv<12>(); bar();
    }
    STAMP(2);
    issue2(3);
    waitv<12>(); bar(); issue2(4);
    waitv<12>(); bar(); issue2(5);
    waitv<12>(); bar();
    waitv<0>(); bar();
    if (b == 0 && lw == 1) {
      const u64* f = flags + (lane >> 4) * 32 + (lane & 8) * 2 + (lane & 7);
#pragma nounroll
      for (int i = 0; i < 4; ++i) poll(f + i * 128, tag);
      if (lane == 0) st_sc1((u64*)ws, tag);
    }
    STAMP(3);
    return;
  }

  char* actp = ws + WS_ACT;
  const bf8 one = ones8();
  u64 tag;
  {
    const int row = P0 + 8 * (grp + (isattn ? 0 : 8)) + w;
    auto rs = __builtin_amdgcn_make_buffer_rsrc((void*)(x + (size_t)row * 640), 0, 1280, 0x00020000);
    bf8 va = __builtin_bit_cast(bf8, __builtin_amdgcn_raw_buffer_load_b128(rs, lane * 16, 0, 0));
    bf8 vb = __builtin_bit_cast(bf8, __builtin_amdgcn_raw_buffer_load_b128(rs, lane * 16 + 1024, 0, 0));
    tag = ld_sc1((const u64*)ws) + 1;
    if (w == 1) *(LAS unsigned*)(lds + AUX_ONES + lane * 4) = 0x3f803f80u;
    float s1 = dot8(vb, one, dot8(va, one, 0.f)), s2 = dot8(vb, vb, dot8(va, va, 0.f));
    float v = red4(s1, s2, 0.f, 0.f);
    v = dpp_add<0xB1>(v); v = dpp_add<0x4E>(v); v = dpp_add<0x141>(v); v = dpp_add<0x140>(v);
    float mean = asf(__builtin_amdgcn_readlane(asu(v), 0)) * (1.f / 640.f);
    float var = fmaxf(asf(__builtin_amdgcn_readlane(asu(v), 16)) * (1.f / 640.f) - mean * mean, 0.f);
    unsigned pkd = pk2(mean, __builtin_amdgcn_sqrtf(bfr(bfr(var) + 1e-5f)));
    if (lane == 0) st_sc1((u64*)(ws + WS_STAT) + row, (tag << 32) | pkd);
    if (w == 0) { *(LAS u64*)(lds + AUX_TAG) = tag; *(LAS unsigned*)(lds + AUX_CNT) = 0; }
  }
  auto rsa = __builtin_amdgcn_make_buffer_rsrc(actp + (size_t)(tb0 + w) * ACT_TB, 0, w < RB ? ACT_TB : 0, 0x00020000);
  u4 R[12];

  if (isattn) {
    const int tau = w >> 2, h = w & 3;
    const int fsw = (c16 >> 1) & 7;
    const int hrow = c16 ^ (tau ? 0 : 8);
    const int xb = (XOFF_A + (16 + 16 * tau + c16) * 128 + ((g ^ fsw) << 4)) ^ (tau << 6);
    int hb = (XOFF_A + hrow * 128 + ((g ^ ((hrow >> 1) & 7)) << 4)) ^ (tau << 6);
    const int hm = c16 != 0;
    if (c16 == 0) hb = AUX_ONES + (hb & 255);
    const int wrow = (16 * (g >> 1) + 4 * (g & 1) + q4) * 480;
    const int wm = wrow + 40 * h + 8 * p4, wr = wrow + 40 * p4 + 32;
    int sg[3];
#pragma unroll
    for (int j = 0; j < 3; ++j) sg[j] = (j + h) % 3;
    f4 Q[4], K[4], V[4], Qh, Kh, Vh, R5[3], X5;
#pragma unroll
    for (int i = 0; i < 4; ++i) { Q[i] = f4{0, 0, 0, 0}; K[i] = Q[i]; V[i] = Q[i]; }
    Qh = Kh = Vh = R5[0] = R5[1] = R5[2] = X5 = Q[0];
    bf8 xf[4], xh, xo, wq, wk, wv, w5[3];
    bar();
    __builtin_amdgcn_s_waitcnt(0xC07F);
    {
      LAS char* px = lds + xb;
      LAS char* pw = lds + tau * 15360 + wm;
      LAS char* pr = lds + tau * 15360 + wr;
#pragma unroll
      for (int j = 0; j < 3; ++j) w5[j] = ldw(pr + sg[j] * 160, 3840);
      xo = ldx(px + h * 4096); SCHED();
      wq = ldw(pw, 3840); SCHED();
      xf[0] = ldx(px); SCHED();
      wk = ldw(pw + 160, 3840); SCHED();
#pragma unroll
      for (int i = 1; i < 4; ++i) { xf[i] = ldx(px + i * 4096); SCHED(); }
      xh = ldx(lds + hb); SCHED();
      wv = ldw(pw + 320, 3840);
    }
    int slot = 0;
#pragma nounroll
    for (int s = 0; s < 10; ++s) {
      const int nslot = slot == 2 * SLOT ? 0 : slot + SLOT;
#pragma unroll
      for (int ks = 0; ks < 2; ++ks) {
        const int ls = ks ? nslot : slot, lk = ks ^ 1;
        LAS char* px = lds + ls + (xb ^ (lk << 6));
        LAS char* pw = lds + ls + (lk ^ tau) * 15360 + wm;
        LAS char* pr = lds + ls + (lk ^ tau) * 15360 + wr;
        LAS char* ph = lds + ((hb + ls * hm) ^ (lk << 6));
        SCHED();
#pragma unroll
        for (int j = 0; j < 3; ++j) R5[j] = mfma(w5[j], xo, R5[j]);
        PIN1(R5[0]); PIN1(R5[1]); PIN1(R5[2]); SCHED();
#pragma unroll
        for (int i = 0; i < 4; ++i) { Q[i] = mfma(wq, xf[i], Q[i]); PIN1(Q[i]); SCHED(); }
        X5 = mfma(w5[0], xh, X5); PIN1(X5);
        if (ks == 0) { Qh = mfma(wq, xh, Qh); PIN1(Qh); }
        SCHED();
        if (ks == 1) bar();
        w5[0] = ldw(pr + sg[0] * 160, 3840); K[0] = mfma(wk, xf[0], K[0]); PIN1(K[0]); SCHED();
        w5[1] = ldw(pr + sg[1] * 160, 3840); K[1] = mfma(wk, xf[1], K[1]); PIN1(K[1]); SCHED();
        w5[2] = ldw(pr + sg[2] * 160, 3840); K[2] = mfma(wk, xf[2], K[2]); PIN1(K[2]); SCHED();
        xo = ldx(px + h * 4096);             K[3] = mfma(wk, xf[3], K[3]); PIN1(K[3]); SCHED();
        wq = ldw(pw, 3840);                  Kh = mfma(wk, xh, Kh); PIN1(Kh); SCHED();
        V[0] = mfma(xf[0], wv, V[0]); PIN1(V[0]); SCHED();
        xf[0] = ldx(px); wk = ldw(pw + 160, 3840);
        V[1] = mfma(xf[1], wv, V[1]); PIN1(V[1]); SCHED();
        xf[1] = ldx(px + 4096);
        V[2] = mfma(xf[2], wv, V[2]); PIN1(V[2]); SCHED();
        xf[2] = ldx(px + 8192);
        V[3] = mfma(xf[3], wv, V[3]); PIN1(V[3]); SCHED();
        xf[3] = ldx(px + 12288);
        Vh = mfma(xh, wv, Vh); PIN1(Vh); SCHED();
        xh = ldx(ph); wv = ldw(pw + 320, 3840);
        SCHED();
      }
      slot = nslot;
    }
    LAS char* cb = lds + AUX_CS;
    if (c16 == 0) {
      *(LAS f4*)(cb + tau * 960 + (20 * h + 4 * g) * 4) = Qh;
      *(LAS f4*)(cb + sg[0] * 320 + (20 * g + 16) * 4) = X5;
    }
    float nm[4], rr[4], nmh, rh, nmo, ro;
    f4 nm4[4], r4[4], nmh4, rh4;
    {
      LAS char* sl = lds + AUX_NM + (16 + 16 * tau + c16) * 4;
      nmo = *(LAS float*)(sl + h * 128); ro = *(LAS float*)(sl + 576 + h * 128);
      nmh = *(LAS float*)(lds + AUX_NM + hrow * 4); rh = *(LAS float*)(lds + AUX_R + hrow * 4);
#pragma unroll
      for (int i = 0; i < 4; ++i) { nm[i] = *(LAS float*)(sl + i * 128); rr[i] = *(LAS float*)(sl + 576 + i * 128); }
    }
    f4 csk;
#pragma unroll
    for (int j = 0; j < 4; ++j) csk[j] = asf(__builtin_amdgcn_update_dpp(0u, asu(Kh[j]), 0x150, 0xf, 0xf, true));
    const float csv = asf(__builtin_amdgcn_ds_bpermute(c16 * 4, asu(Vh[0])));
    STAMP(1);
    bar();
    u2 Qp[4], Kp[5], Vp[5];
    LAS char* rb = lds + SC_REM + tau * 7680 + c16 * 32 + g * 8;
    {
      f4 c5[3];
#pragma unroll
      for (int j = 0; j < 3; ++j) c5[j] = *(LAS f4*)(cb + sg[j] * 320 + (20 * g + 16) * 4);
      f4 csq = *(LAS f4*)(cb + (20 * h + 4 * g) * 4), csq2 = *(LAS f4*)(cb + 960 + (20 * h + 4 * g) * 4);
      SCHED();
      Kp[0] = pk4(Kh * rh + csk * nmh);
#pragma unroll
      for (int i = 0; i < 4; ++i) Kp[i + 1] = pk4(K[i] * rr[i] + csk * nm[i]);
      PIN4(Kp[1], Kp[2], Kp[3], Kp[4]); PIN1(Kp[0]);
      SCHED();
#pragma unroll
      for (int j = 0; j < 3; ++j) *(LAS u2*)(rb + sg[j] * 2560 + (h + 1) * 512) = pk4(R5[j] * ro + c5[j] * nmo);
      *(LAS u2*)(rb + sg[0] * 2560) = pk4(X5 * rh + c5[0] * nmh);
      SCHED();
      LAS char* sr = lds + AUX_NM + (16 + 16 * tau + 4 * g) * 4;
      const int h4 = (4 * g) ^ (tau ? 0 : 8);
      nmh4 = *(LAS f4*)(lds + AUX_NM + h4 * 4); rh4 = *(LAS f4*)(lds + AUX_R + h4 * 4);
#pragma unroll
      for (int i = 0; i < 4; ++i) { nm4[i] = *(LAS f4*)(sr + i * 128); r4[i] = *(LAS f4*)(sr + 576 + i * 128); }
      SCHED();
      csq += csq2;
#pragma unroll
      for (int i = 0; i < 4; ++i) Qp[i] = pk4(Q[i] * rr[i] + csq * nm[i]);
      PIN4(Qp[0], Qp[1], Qp[2], Qp[3]);
    }
    STAMP(4);
    bar();
    {
      LAS char* rt = lds + SC_REM + tau * 7680 + 5120 + lane * 8;
      u2 K5p[5], V5p[5], q5[4];
#pragma unroll
      for (int i = 0; i < 5; ++i) {
        K5p[i] = *(LAS u2*)(rb + 2560 + i * 512);
        if (i < 4) q5[i] = *(LAS u2*)(rb + (i + 1) * 512);
      }
#pragma unroll
      for (int i = 0; i < 5; ++i) V5p[i] = __builtin_bit_cast(u2, __builtin_amdgcn_ds_read_tr16_b64_v4bf16((LAS bf4*)(rt + i * 512)));
      SCHED();
      Vp[0] = pk4(Vh * rh4 + nmh4 * csv);
#pragma unroll
      for (int i = 0; i < 4; ++i) Vp[i + 1] = pk4(V[i] * r4[i] + nm4[i] * csv);
      PIN4(Vp[1], Vp[2], Vp[3], Vp[4]); PIN1(Vp[0]);
      f4 mcur, mprev; u2 bcur;
#pragma unroll
      for (int j = 0; j < 4; ++j) {
        int d = c16 - 4 * g - j;
        mcur[j] = (unsigned)d < 8u ? 0.f : -1e30f;
        mprev[j] = d < -8 ? 0.f : -1e30f;
      }
#pragma unroll
      for (int j = 0; j < 2; ++j) {
        int key = 4 * g + 2 * j;
        bcur[j] = (key <= c16 ? 0xffffu : 0u) | (key + 1 <= c16 ? 0xffff0000u : 0u);
      }
      SCHED();
#pragma unroll
      for (int ks = 0; ks < 12; ++ks) R[ks] = __builtin_amdgcn_raw_buffer_load_b128(rsa, lane * 16, ks * 1024, 16);
      SCHED();
      const float cexp = 0.22360679774997896f * 1.4426950408889634f;
      f4 sc[4], sp[4];
#pragma unroll
      for (int i = 0; i < 4; ++i) {
        if (g != h) q5[i] = u2{0, 0};
        bf8 qb = cat(Qp[i], q5[i]);
        f4 mp = mprev;
        if (i == 0 && panel == 0) mp = f4{-1e30f, -1e30f, -1e30f, -1e30f};
        sc[i] = mfma(cat(Kp[i + 1], K5p[i + 1]), qb, mcur);
        sp[i] = mfma(cat(Kp[i], K5p[i]), qb, mp);
      }
      bf8 pb[4];
#pragma unroll
      for (int i = 0; i < 4; ++i) {
        f4 sv;
#pragma unroll
        for (int j = 0; j < 4; ++j) sv[j] = fmx(sc[i][j], sp[i][j]);
        float mc = xg_max(fmx(fmx(sv[0], sv[1]), fmx(sv[2], sv[3]))) * cexp;
        f4 pv;
#pragma unroll
        for (int j = 0; j < 4; ++j) pv[j] = __builtin_amdgcn_exp2f(sv[j] * cexp - mc);
        u2 pp = pk4(pv);
        pb[i] = cat(pp & bcur, pp & ~bcur);
      }
      LAS char* ob = lds + SC_O + (2 * c16 + tau) * OPITCH + 40 * h;
      LAS char* ob5 = g == h ? ob + 32 : lds + SC_O + 160;
      const f4 zero = {0, 0, 0, 0};
#pragma unroll
      for (int i = 0; i < 4; ++i) {
        f4 l = mfma(one, pb[i], zero);
        f4 o = mfma(cat(Vp[i + 1], Vp[i]), pb[i], zero);
        f4 o5 = mfma(cat(V5p[i + 1], V5p[i]), pb[i], zero);
        float inv = __builtin_amdgcn_rcpf(l[0]);
        *(LAS u2*)(ob + i * 32 * OPITCH + 8 * g) = pk4(o * inv);
        *(LAS u2*)(ob5 + i * 32 * OPITCH) = pk4(o5 * inv);
      }
    }
    STAMP(5);
    bar();
    if (w >= 4) {
#pragma unroll
      for (int i = 0; i < 2; ++i) {
        const int tl = 2 * (w - 4) + i;
        LAS char* op = lds + SC_O + (16 * tl + c16) * OPITCH + 16 * g;
        u4 f0 = *(LAS u4*)op, f1 = *(LAS u4*)(op + 64), f2v = *(LAS u4*)(op + (g < 2 ? 128 : 0));
        auto rs = __builtin_amdgcn_make_buffer_rsrc(actp + (size_t)(panel * 8 + tl) * ACT_TB + (48 + 3 * grp) * 1024, 0, 2560, 0x00020000);
        __builtin_amdgcn_raw_buffer_store_b128(f0, rs, lane * 16, 0, 16);
        __builtin_amdgcn_raw_buffer_store_b128(f1, rs, lane * 16, 1024, 16);
        __builtin_amdgcn_raw_buffer_store_b128(f2v, rs, lane * 16 + 2048, 0, 16);
      }
      waitv<0>();
      if (lane == 0 && __hip_atomic_fetch_add((LAS unsigned*)(lds + AUX_CNT), 1u, __ATOMIC_RELAXED, __HIP_MEMORY_SCOPE_WORKGROUP) == 3)
        st_sc1(flags + panel * 32 + 16 + grp, tag);
      STAMP(2);
    }
  } else {
    const int tq = w & 3, fh = w >> 2;
    const int rho = 4 * (g & 1) + q4;
    const int role = (tq + 2 * fh) & 3;
    const int xb = XOFF_F + (32 * tq + c16) * 128 + ((g ^ ((c16 >> 1) & 7)) << 4);
    const int wl = (g >> 1) * 2048 + rho * 128 + (((p4 >> 1) ^ (rho & 6)) << 4) + (p4 & 1) * 8;
    int wb[3], pu[3];
    u2 bias[6];
#pragma unroll
    for (int u = 0; u < 3; ++u) {
      pu[u] = 3 * fh + (u + role) % 3;
      wb[u] = (wl ^ ((pu[u] & 1) << 6)) + (pu[u] >> 1) * 4096;
      bias[2 * u] = *(const u2*)(b1 + grp * 192 + pu[u] * 32 + 4 * g);
      bias[2 * u + 1] = *(const u2*)(b1 + grp * 192 + pu[u] * 32 + 16 + 4 * g);
    }
    f4 acc[2][6], CS[2];
#pragma unroll
    for (int e = 0; e < 6; ++e) { acc[0][e] = f4{0, 0, 0, 0}; acc[1][e] = acc[0][e]; }
    CS[0] = CS[1] = acc[0][0];
    bf8 onev = one;
    asm volatile("" : "+v"(onev));
    bf8 xf[2][2], wf[6];
    bar();
    __builtin_amdgcn_s_waitcnt(0xC07F);
    xf[0][0] = ldx(lds + xb); xf[0][1] = ldx(lds + xb + 2048);
#pragma unroll
    for (int e = 0; e < 6; ++e) wf[e] = ldw(lds + (wb[e >> 1] ^ ((e & 1) << 5)), 1024);
    int slot = 0;
#pragma nounroll
    for (int s = 0; s < 10; ++s) {
      const int nslot = slot == 2 * SLOT ? 0 : slot + SLOT;
#pragma unroll
      for (int ks = 0; ks < 2; ++ks) {
        const int lk = ks ^ 1;
        LAS char* sp = lds + (ks ? nslot : slot);
        SCHED();
#pragma unroll
        for (int e = 0; e < 2; ++e) {
          acc[0][e] = mfma(wf[e], xf[ks][0], acc[0][e]); acc[1][e] = mfma(wf[e], xf[ks][1], acc[1][e]);
          PIN1(acc[0][e]); PIN1(acc[1][e]); SCHED();
        }
        asm volatile("s_cmp_eq_u32 %5, 3\n\ts_cbranch_scc1 .Lcs%=\n\tv_mfma_f32_16x16x32_bf16 %0, %2, %4, %0\n\tv_mfma_f32_16x16x32_bf16 %1, %3, %4, %1\n.Lcs%=:"
                     : "+v"(CS[0]), "+v"(CS[1]) : "v"(wf[0]), "v"(wf[1]), "v"(onev), "s"(role) : "scc");
        SCHED();
        if (ks == 1) bar();
#pragma unroll
        for (int e = 2; e < 6; ++e) {
          if (e == 2) { xf[lk][0] = ldx(sp + (xb ^ (lk << 6))); xf[lk][1] = ldx(sp + (xb ^ (lk << 6)) + 2048); }
          else wf[e - 1] = ldw(sp + (wb[(e - 1) >> 1] ^ (((e - 1) & 1) << 5)) + lk * 12288, 1024);
          acc[0][e] = mfma(wf[e], xf[ks][0], acc[0][e]); PIN1(acc[0][e]); SCHED();
          if (e < 4) wf[e - 2] = ldw(sp + (wb[0] ^ ((e & 1) << 5)) + lk * 12288, 1024);
          acc[1][e] = mfma(wf[e], xf[ks][1], acc[1][e]); PIN1(acc[1][e]); SCHED();
        }
        wf[5] = ldw(sp + (wb[2] ^ 32) + lk * 12288, 1024);
        SCHED();
      }
      slot = nslot;
    }
    asm volatile("s_nop 7" : "+v"(CS[0]), "+v"(CS[1]));
    if (role < 3 && c16 < 2) *(LAS f4*)(lds + AUX_CS + (pu[0] * 32 + c16 * 16 + 4 * g) * 4) = c16 ? CS[1] : CS[0];
    float nm[2], rr[2];
#pragma unroll
    for (int i = 0; i < 2; ++i) { nm[i] = *(LAS float*)(lds + AUX_NM + (32 * tq + 16 * i + c16) * 4); rr[i] = *(LAS float*)(lds + AUX_R + (32 * tq + 16 * i + c16) * 4); }
    STAMP(1);
    bar();
    f4 cb4[6];
#pragma unroll
    for (int e = 0; e < 6; ++e) cb4[e] = *(LAS f4*)(lds + AUX_CS + (pu[e >> 1] * 32 + (e & 1) * 16 + 4 * g) * 4);
#pragma unroll
    for (int i = 0; i < 2; ++i) {
      const int tl = 2 * tq + i;
      auto rs = __builtin_amdgcn_make_buffer_rsrc(actp + (size_t)(panel * 8 + tl) * ACT_TB + 6 * grp * 1024, 0, 6144, 0x00020000);
#pragma unroll
      for (int u = 0; u < 3; ++u) {
        u2 hp[2];
#pragma unroll
        for (int d = 0; d < 2; ++d) {
          const int e = 2 * u + d;
          f4 bb = {lo16(bias[e][0]), hi16(bias[e][0]), lo16(bias[e][1]), hi16(bias[e][1])};
          f4 v = acc[i][e] * rr[i] + (cb4[e] * nm[i] + bb);
#pragma unroll
          for (int j = 0; j < 4; ++j) v[j] = v[j] * fmx(v[j], 0.f);
          hp[d] = pk4(v);
        }
        __builtin_amdgcn_raw_buffer_store_b128(u4{hp[0][0], hp[0][1], hp[1][0], hp[1][1]}, rs, lane * 16, pu[u] * 1024, 16);
      }
    }
    STAMP(6);
    bar();
    waitv<0>();
    if (lane == 0 && __hip_atomic_fetch_add((LAS unsigned*)(lds + AUX_CNT), 1u, __ATOMIC_RELAXED, __HIP_MEMORY_SCOPE_WORKGROUP) == 7)
      st_sc1(flags + panel * 32 + grp, tag);
    STAMP(2);
    if (w == 7) poll(myflag, tag);
    bar();
#pragma unroll
    for (int ks = 0; ks < 12; ++ks) R[ks] = __builtin_amdgcn_raw_buffer_load_b128(rsa, lane * 16, ks * 1024, 16);
  }

  if (w >= RB) {
    bar(); bar();
    if (w == 7) poll(myflag + 16, tag);
    bar(); bar(); bar();
    return;
  }
  {
    STAMP(7);
    const int tb = tb0 + w;
    const int rho = 4 * (g & 1) + q4;
    const int wl = (g >> 1) * 2048 + rho * 128 + (((p4 >> 1) ^ (rho & 6)) << 4) + (p4 & 1) * 8;
    u2 bias[4];
#pragma unroll
    for (int c = 0; c < 4; ++c) bias[c] = *(const u2*)(b2 + ct * 64 + c * 16 + 4 * g);
    const size_t xo = (size_t)(tb * 16 + c16) * 640 + ct * 64 + g * 16;
    const u4 xr0 = *(const u4*)(x + xo), xr1 = *(const u4*)(x + xo + 8);
    f4 acc[4], gate[4];
#pragma unroll
    for (int c = 0; c < 4; ++c) { acc[c] = f4{0, 0, 0, 0}; gate[c] = acc[c]; }
    unsigned va = lane * 16, va3 = va;
    int slot = SLOT;
#pragma nounroll
    for (int G = 0; G < 6; ++G) {
      if (G > 0) bar();
      if (G == 1) STAMP(8);
      if (G == 2) STAMP(9);
      if (G == 3) STAMP(10);
      if (G == 4) STAMP(11);
      if (G == 5) STAMP(12);
      va += 12288;
      va3 = (G >= 3 && lane >= 32) ? 0x80000000u : va;
      LAS char* sp = lds + slot;
      bf8 wf[2][4];
#pragma unroll
      for (int c = 0; c < 4; ++c) wf[0][c] = ldw(sp + (wl ^ (c << 5)), 1024);
      SCHED();
#pragma unroll
      for (int ks = 0; ks < 12; ++ks) {
        bf8 a = __builtin_bit_cast(bf8, R[ks]);
#pragma unroll
        for (int c = 0; c < 4; ++c) {
          if (ks < 11) wf[(ks + 1) & 1][c] = ldw(sp + (wl ^ (c << 5)) + (ks + 1) * 4096, 1024);
          acc[c] = mfma(wf[ks & 1][c], a, acc[c]);
          asm volatile("" : "+v"(acc[c]));
          SCHED();
        }
        R[ks] = __builtin_amdgcn_raw_buffer_load_b128(rsa, (ks % 3 == 2) ? va3 : va, ks * 1024, 16);
        SCHED();
      }
      if (G == 3) {
#pragma unroll
        for (int c = 0; c < 4; ++c) {
          f4 bb = {lo16(bias[c][0]), hi16(bias[c][0]), lo16(bias[c][1]), hi16(bias[c][1])};
          f4 f = acc[c] + bb;
#pragma unroll
          for (int j = 0; j < 4; ++j) gate[c][j] = f[j] * __builtin_amdgcn_rcpf(1.f + __builtin_amdgcn_exp2f(f[j] * -1.4426950408889634f));
          acc[c] = f4{0, 0, 0, 0};
        }
      }
      slot = slot == 2 * SLOT ? 0 : slot + SLOT;
    }
    u2 Y[4];
#pragma unroll
    for (int c = 0; c < 4; ++c) Y[c] = pk4(acc[c] * gate[c]);
#pragma unroll
    for (int d = 0; d < 2; ++d) {
      auto s0 = __builtin_amdgcn_permlane32_swap(Y[0][d], Y[2][d], false, false);
      auto s1 = __builtin_amdgcn_permlane32_swap(Y[1][d], Y[3][d], false, false);
      auto t0 = __builtin_amdgcn_permlane16_swap(s0[0], s1[0], false, false);
      auto t1 = __builtin_amdgcn_permlane16_swap(s0[1], s1[1], false, false);
      Y[0][d] = t0[0]; Y[1][d] = t0[1]; Y[2][d] = t1[0]; Y[3][d] = t1[1];
    }
    u4 o0, o1;
#pragma unroll
    for (int d = 0; d < 4; ++d) {
      unsigned y0 = Y[d >> 1][d & 1], y1 = Y[2 + (d >> 1)][d & 1];
      o0[d] = pk2(lo16(xr0[d]) + lo16(y0), hi16(xr0[d]) + hi16(y0));
      o1[d] = pk2(lo16(xr1[d]) + lo16(y1), hi16(xr1[d]) + hi16(y1));
    }
    *(u4*)(out + xo) = o0;
    *(u4*)(out + xo + 8) = o1;
    STAMP(3);
  }
}

__global__ void diag_spin(char* ws) {
  const u64* s = (const u64*)(ws + WS_STAMP);
  int l = threadIdx.x;
  __shared__ u64 sh[256], sv[256];
  sh[l] = s[(l * 12) * 16];
  bool use = (0 == 2) || (((l >> 3) >= 16) == (0 == 1));
  sv[l] = use ? s[(l * 12 + 8) * 16 + 10] : 0;
  __syncthreads();
  if (l == 0) {
    u64 t0 = ~0ull; for (int i = 0; i < 256; ++i) t0 = sh[i] < t0 ? sh[i] : t0;
    u64 acc = 0, n = 0, mx = 0; for (int i = 0; i < 256; ++i) if (sv[i]) { acc += sv[i] - t0; n++; mx = sv[i] - t0 > mx ? sv[i] - t0 : mx; }
    u64 d = 0 ? mx : acc / n;
    if (d > 10000) d = 10000;
    u64 a = __builtin_amdgcn_s_memrealtime();
    while (__builtin_amdgcn_s_memrealtime() - a < d * 100) __builtin_amdgcn_s_sleep(8);
  }
}
extern "C" void kernel_launch(void* const* d_in, const int* in_sizes, int n_in, void* d_out, int out_size, void* d_ws, size_t ws_size,
                              hipStream_t stream) {
  (void)in_sizes; (void)n_in; (void)out_size; (void)ws_size;
  (void)hipFuncSetAttribute((const void*)fused, hipFuncAttributeMaxDynamicSharedMemorySize, LDS_BYTES);
  fused<<<256, 768, LDS_BYTES, stream>>>((const u16*)d_in[0], (const u16*)d_in[1], (const u16*)d_in[2], (const u16*)d_in[4],
                                         (const u16*)d_in[5], (const u16*)d_in[6], (const u16*)d_in[7], (u16*)d_out, (char*)d_ws);
  diag_spin<<<1, 256, 0, stream>>>((char*)d_ws);
}
```

```cpp
#include <hip/hip_runtime.h>
#include <stdint.h>
#include <type_traits>

typedef unsigned short u16;
typedef unsigned long long u64;
typedef __bf16 bf8 __attribute__((ext_vector_type(8)));
typedef __bf16 bf4 __attribute__((ext_vector_type(4)));
typedef __bf16 bf2 __attribute__((ext_vector_type(2)));
typedef float f4 __attribute__((ext_vector_type(4)));
typedef float f2 __attribute__((ext_vector_type(2)));
typedef unsigned u4 __attribute__((ext_vector_type(4)));
typedef unsigned u2 __attribute__((ext_vector_type(2)));
typedef __amdgpu_buffer_rsrc_t rsrc_t;
#define LAS __attribute__((address_space(3)))
#define DI __device__ __forceinline__
#define INL __attribute__((always_inline))

#define STAMPS 0
#define ABL 0
#define PRIO 3
#define HOT 0
#define PRIOY 0
#define PRIO2 0
#define SPLITPOLL 1
#define NF0 1
#define LFIRST 1
#define CPF 1
#define RBF 6
#define TOUCH 1
#define ROT 0
#define ROT2 (SPLITPOLL ? 2 * half : 0)

constexpr int SLOT = 49152;
constexpr int AUX = 3 * SLOT;
constexpr int AUX_ONES = AUX;
constexpr int AUX_NM = AUX + 1024;
constexpr int AUX_R = AUX_NM + 576;
constexpr int AUX_CS = AUX_NM + 1152;
constexpr int AUX_TAG = AUX_CS + 1280;
constexpr int AUX_CNT = AUX_TAG + 8;
constexpr int AUX_RDY = AUX_TAG + 12;
constexpr int AUX_DUMMY = AUX_TAG + 16;
constexpr int AUX_MASK = AUX_DUMMY + 1024;
constexpr int AUX_ST = AUX_MASK + 4096;
constexpr int LDS_BYTES = AUX_ST + (STAMPS ? 1536 : 0);
constexpr int XOFF_A = 30720;
constexpr int XOFF_F = 24576;
constexpr int SC_REM = 0;
constexpr int SC_O = 16384;
constexpr int OPITCH = 176;

constexpr int WS_FLAG = 4096;
constexpr int WS_STAT = 16384;
constexpr int WS_STAMP = 65536;
constexpr size_t WS_ACT = 1 << 20;
constexpr int ACT_TB = 72 * 1024;
constexpr int RBA = 10 - RBF;
constexpr unsigned OOB = 0x80000000u;
constexpr unsigned CODE_BYTES = 28672;

DI void bar() { asm volatile("s_waitcnt lgkmcnt(0)\n\ts_barrier" ::: "memory"); }
template <int N> DI void waitv() { asm volatile("s_waitcnt vmcnt(%0)" ::"i"(N) : "memory"); }
DI f4 mfma(bf8 a, bf8 b, f4 c) { return __builtin_amdgcn_mfma_f32_16x16x32_bf16(a, b, c, 0, 0, 0); }
DI unsigned rfl(unsigned v) { return __builtin_amdgcn_readfirstlane(v); }
DI rsrc_t mkrs(const void* p, unsigned bytes) { return __builtin_amdgcn_make_buffer_rsrc((void*)p, 0, bytes, 0x00020000); }
#define DMA_L(V, OFF) "buffer_load_dwordx4 " V ", %[rs], %[so] offen offset:" OFF " lds\n\t"
template <int STEP> DI void dma4(unsigned ldsaddr, const unsigned* v, rsrc_t rs, unsigned soff) {
  if (ABL & 1) return;
  asm volatile("s_mov_b32 m0, %[l]\n\ts_nop 0\n\t" DMA_L("%[a]", "0") DMA_L("%[b]", "%[o1]") DMA_L("%[c]", "%[o2]") DMA_L("%[d]", "%[o3]")
               :: [l] "s"(ldsaddr), [a] "v"(v[0]), [b] "v"(v[1]), [c] "v"(v[2]), [d] "v"(v[3]), [rs] "s"(rs), [so] "s"(soff),
                  [o1] "i"(STEP), [o2] "i"(2 * STEP), [o3] "i"(3 * STEP) : "memory");
}
template <int STEP> DI void dma2(unsigned ldsaddr, const unsigned* v, rsrc_t rs, unsigned soff) {
  if (ABL & 1) return;
  asm volatile("s_mov_b32 m0, %[l]\n\ts_nop 0\n\t" DMA_L("%[a]", "0") DMA_L("%[b]", "%[o1]")
               :: [l] "s"(ldsaddr), [a] "v"(v[0]), [b] "v"(v[1]), [rs] "s"(rs), [so] "s"(soff), [o1] "i"(STEP) : "memory");
}
DI void dma1(unsigned ldsaddr, unsigned v, rsrc_t rs, unsigned soff) {
  if (ABL & 1) return;
  asm volatile("s_mov_b32 m0, %[l]\n\ts_nop 0\n\t" DMA_L("%[a]", "0") :: [l] "s"(ldsaddr), [a] "v"(v), [rs] "s"(rs), [so] "s"(soff) : "memory");
}
DI void dma8x60(unsigned ldsaddr, const unsigned* v, rsrc_t rs, unsigned soff) {
  if (ABL & 1) return;
  asm volatile("s_mov_b32 m0, %[l]\n\ts_mov_b32 exec_hi, 0x0fffffff\n\t" DMA_L("%[a]", "0") DMA_L("%[b]", "960") DMA_L("%[c]", "1920") DMA_L("%[d]", "2880")
               "s_add_u32 m0, m0, 3840\n\ts_nop 0\n\t" DMA_L("%[e]", "0") DMA_L("%[f]", "960") DMA_L("%[g]", "1920") DMA_L("%[h]", "2880") "s_mov_b32 exec_hi, -1"
               :: [l] "s"(ldsaddr), [a] "v"(v[0]), [b] "v"(v[1]), [c] "v"(v[2]), [d] "v"(v[3]), [e] "v"(v[4]), [f] "v"(v[5]), [g] "v"(v[6]), [h] "v"(v[7]),
                  [rs] "s"(rs), [so] "s"(soff) : "memory", "scc");
}
DI rsrc_t mkrs4k(const void* p, unsigned bytes) { return mkrs((const char*)p - 4096, bytes + 4096); }
DI bf8 ldx(LAS char* p) { return *(LAS bf8*)p; }
DI bf4 ldt(LAS char* p) { return __builtin_amdgcn_ds_read_tr16_b64_v4bf16((LAS bf4*)p); }
template <bool SC1> DI void st128(u4 v, rsrc_t rs, unsigned voff, unsigned soff) {
  if (SC1) asm volatile("buffer_store_dwordx4 %0, %1, %2, %3 offen sc1\n\ts_nop 1" :: "v"(v), "v"(voff), "s"(rs), "s"(soff) : "memory");
  else asm volatile("buffer_store_dwordx4 %0, %1, %2, %3 offen\n\ts_nop 1" :: "v"(v), "v"(voff), "s"(rs), "s"(soff) : "memory");
}
DI LAS char* LP(int a) { return (LAS char*)(unsigned)a; }
DI bf8 ldw(LAS char* p, int tstride) { return __builtin_shufflevector(ldt(p), ldt(p + tstride), 0, 1, 2, 3, 4, 5, 6, 7); }
DI float dot8(bf8 a, bf8 b, float c) {
  c = __builtin_amdgcn_fdot2_f32_bf16(__builtin_shufflevector(a, a, 0, 1), __builtin_shufflevector(b, b, 0, 1), c, false);
  c = __builtin_amdgcn_fdot2_f32_bf16(__builtin_shufflevector(a, a, 2, 3), __builtin_shufflevector(b, b, 2, 3), c, false);
  c = __builtin_amdgcn_fdot2_f32_bf16(__builtin_shufflevector(a, a, 4, 5), __builtin_shufflevector(b, b, 4, 5), c, false);
  c = __builtin_amdgcn_fdot2_f32_bf16(__builtin_shufflevector(a, a, 6, 7), __builtin_shufflevector(b, b, 6, 7), c, false);
  return c;
}
DI bf8 asb(u4 v) { return __builtin_bit_cast(bf8, v); }
DI bf8 ones8() { return asb(u4{0x3f803f80u, 0x3f803f80u, 0x3f803f80u, 0x3f803f80u}); }
DI float asf(unsigned u) { return __builtin_bit_cast(float, u); }
DI unsigned asu(float f) { return __builtin_bit_cast(unsigned, f); }
DI unsigned pk2(float a, float b) { f2 v = {a, b}; return __builtin_bit_cast(unsigned, __builtin_convertvector(v, bf2)); }
DI u2 pk4(f4 v) { u2 r = {pk2(v[0], v[1]), pk2(v[2], v[3])}; return r; }
DI float bfr(float a) { return asf(pk2(a, 0.f) << 16); }
DI float lo16(unsigned u) { return asf(u << 16); }
DI float hi16(unsigned u) { return asf(u & 0xffff0000u); }
DI f4 unpk4(u2 v) { return f4{lo16(v[0]), hi16(v[0]), lo16(v[1]), hi16(v[1])}; }
DI bf8 cat(u2 a, u2 b) { return asb(u4{a[0], a[1], b[0], b[1]}); }
DI float fmx(float a, float b) { return __builtin_elementwise_maximum(a, b); }
template <int CTRL> DI float dpp_add(float v) { return v + asf(__builtin_amdgcn_update_dpp(0u, asu(v), CTRL, 0xf, 0xf, true)); }
#define SWAP16(a, b) __builtin_amdgcn_permlane16_swap(a, b, false, false)
#define SWAP32(a, b) __builtin_amdgcn_permlane32_swap(a, b, false, false)
#define PIN4(a, b, c, d) asm volatile("" : "+v"(a), "+v"(b), "+v"(c), "+v"(d))
#define PIN1(a) asm volatile("" : "+v"(a))
#define SCHED() __builtin_amdgcn_sched_barrier(0)
DI u2 ld_ctl(rsrc_t rc, unsigned off) { asm volatile("" ::: "memory"); return __builtin_amdgcn_raw_buffer_load_b64(rc, off, 0, 16); }
DI void st_ctl(rsrc_t rc, int lane, unsigned off, unsigned lo, unsigned hi) {
  __builtin_amdgcn_raw_buffer_store_b64(u2{lo, hi}, rc, lane == 0 ? off : OOB, 0, 16);
  asm volatile("" ::: "memory");
}
DI void poll(rsrc_t rc, unsigned off, unsigned tlo, unsigned thi) {
#pragma nounroll
  for (int it = 0; it < (1 << 16); ++it) {
    u2 v = ld_ctl(rc, off);
    if (__all(v[0] == tlo && v[1] == thi)) break;
    __builtin_amdgcn_s_sleep(2);
  }
  asm volatile("" ::: "memory");
}
DI unsigned arrive(unsigned ldsaddr) {
  unsigned r, one = 1;
  asm volatile("s_mov_b64 exec, 1\n\tds_add_rtn_u32 %0, %1, %2\n\ts_waitcnt lgkmcnt(0)\n\ts_mov_b64 exec, -1" : "=&v"(r) : "v"(ldsaddr), "v"(one) : "memory");
  return rfl(r);
}
#define STAMP(i)
#define STAMP_INIT()
#define STAMP_FLUSH()

__global__ __launch_bounds__(768, 3) void fused(const u16* __restrict__ x, const u16* __restrict__ wqkv, const u16* __restrict__ wo,
                                                const u16* __restrict__ w1, const u16* __restrict__ b1, const u16* __restrict__ w2,
                                                const u16* __restrict__ b2, u16* __restrict__ out, char* __restrict__ ws) {
  extern __shared__ __attribute__((aligned(16))) char smem[];
  LAS char* lds = (LAS char*)smem;
  const unsigned ldsb = (unsigned)(size_t)lds;
  const int tid = threadIdx.x, lane = tid & 63, w = rfl(tid >> 6);
  unsigned cpf = 0;
  if (CPF) {
    u64 pc;
    asm volatile("s_getpc_b64 %0" : "=s"(pc));
    if (w < 4) cpf = __builtin_amdgcn_raw_buffer_load_b32(mkrs((const void*)pc, CODE_BYTES), tid * 128 + ((blockIdx.x >> 3) & 3 ? OOB : 0), 0, 0);
  }
  const int b = blockIdx.x, xcd = b & 7, loc = b >> 3;
  const int rq = xcd >> 1, half = xcd & 1;
  const bool isattn = loc < 16;
  const int li = loc & 15;
  const int panel = 4 * rq + (li & 3);
  const int grp = 4 * half + (li >> 2);
  const int P0 = panel * 128;
  const int rot = ROT;
  int ctl, rb0;
  if (isattn) { if (li < 6) { ctl = li >> 1; rb0 = 24 + 4 * (li & 1); } else { ctl = 3 + (li - 6) / 5; rb0 = 12 + 4 * ((li - 6) % 5); } }
  else        { if (li < 12) { ctl = li >> 2; rb0 = 6 * (li & 3); } else { ctl = 3 + ((li - 12) >> 1); rb0 = 6 * (li & 1); } }
  const int ct = 5 * half + ctl;
  const int tb0 = rq * 32 + rb0;
  const int RB = isattn ? RBA : RBF;
  const int rot2 = ROT2;
  auto gphys = [&](int G) INL { return G < 4 ? (G + rot2) & 3 : G < 6 ? 4 + ((G + (rot2 >> 1)) & 1) : 6; };
  const int g = lane >> 4, c16 = lane & 15, q4 = (lane >> 2) & 3, p4 = lane & 3;
  const rsrc_t rc = mkrs(ws, 65536);
  auto myflag = [&]() INL { return WS_FLAG + ((lane & 8) ? (tb0 + RB - 1) >> 3 : tb0 >> 3) * 256 + (lane & 7) * 8; };
  auto myflag1 = [&]() INL { return SPLITPOLL ? (myflag() & ~32) | (half << 5) : myflag(); };
  char* actp = ws + WS_ACT;
  STAMP_INIT();
  STAMP(0);

  if (__builtin_expect(w >= 8, LFIRST)) {
    __builtin_amdgcn_s_setprio(PRIO);
    const int lw = w - 8;
    const int rho = lane >> 3, cs8 = lane & 7, hh = lw >> 1, tt = lw & 1;
    const rsrc_t rsx = mkrs4k(x, 2048 * 1280);
    const rsrc_t rsw = isattn ? mkrs4k(wqkv, 640 * 3840) : mkrs4k(w1, 640 * 3072);
    const unsigned swz = (cs8 ^ (rho & 6)) << 4;
    unsigned vx[5], vw[8];
    {
      const int r8 = (lane >> 2) & 7, r2 = r8 >> 2;
      const unsigned col = (lane >> 5) * 64 + 4096;
#pragma unroll
      for (int i = 0; i < 4; ++i) {
        const int row = isattn ? P0 + 32 * lw + 16 * (i & 1) + (i >> 1) + 2 * r8 : P0 + 32 * lw + 8 * i + r8;
        vx[i] = row * 1280 + col + ((p4 ^ ((-(2 * (i & 1) + r2)) & 3)) << 4) - i * 1024;
      }
      vx[4] = max(P0 - 16 + tt + 2 * r8, 0) * 1280 + col + ((p4 ^ ((-(2 * tt + r2)) & 3)) << 4);
    }
    if (isattn) {
      const int r2 = lane >= 30, cc = lane - 30 * r2, seg = (cc >= 10) + (cc >= 20);
      const unsigned base = (16 * lw + r2) * 3840 + seg * 1120 + cc * 16 + grp * 160 + 4096;
#pragma unroll
      for (int i = 0; i < 8; ++i) vw[i] = base + (4 * (i >> 2) + 8 * ((i >> 1) & 1) + 2 * (i & 1)) * 3840 - (i & 3) * 960;
    } else {
      const unsigned base = (16 * hh + 4 * tt + (rho >> 2) * 8 + (rho & 3)) * 3072 + grp * 384 + swz + 4096;
#pragma unroll
      for (int i = 0; i < 6; ++i) vw[i] = base + (i / 3) * 32 * 3072 + (i % 3) * 128 - (i & 3) * 1024;
      vw[6] = vw[7] = 0;
    }
    const bool ext = isattn && lw < 2;
    asm volatile("s_nop 4" ::: "memory");

    auto issue1a = [&](int t) INL {
      int kt = t + rot; kt = kt >= 10 ? kt - 10 : kt; if (HOT) kt = kt & 1;
      const unsigned sb = ldsb + (t % 3) * SLOT;
      if (!(ABL & 16)) dma4<1024>(sb + (isattn ? XOFF_A + 2048 : XOFF_F) + lw * 4096, vx, rsx, kt * 128);
      if (ext) dma1(sb + XOFF_A + lw * 1024, vx[4], rsx, kt * 128);
    };
    auto issue1b = [&](int t) INL {
      int kt = t + rot; kt = kt >= 10 ? kt - 10 : kt; if (HOT) kt = kt & 1;
      const unsigned sb = ldsb + (t % 3) * SLOT;
      if (ABL & 32) return;
      if (isattn) {
        dma8x60(sb + lw * 7680, vw, rsw, kt * (64 * 3840));
      } else {
        dma4<1024>(sb + lw * 6144, vw, rsw, kt * (64 * 3072));
        dma2<1024>(sb + lw * 6144 + 4096, vw + 4, rsw, kt * (64 * 3072));
      }
    };
    STAMP(10);
    issue1a(0); issue1b(0); issue1a(1);
    const rsrc_t rs2 = mkrs(w2, 1536 * 1280), rso = mkrs(wo, 640 * 1280);
    const unsigned v2h = (16 * tt + 8 * hh + rho) * 1280 + ct * 128 + swz;
    const unsigned v2o = (16 * hh + 8 * (rho >> 2) + 4 * tt + (rho & 3)) * 1280 + ct * 128 + swz;
    const unsigned v2o3 = hh ? OOB : v2o;
    unsigned v2d[4];
#pragma unroll
    for (int i = 0; i < 4; ++i) v2d[i] = v2h + i * (32 * 1280 - 1024);
    auto issue2 = [&](int G) INL {
      const unsigned sb = ldsb + ((G + 1) % 3) * SLOT + lw * 12288, so = gphys(G) * 384 * 1280;
#pragma unroll
      for (int a = 0; a < 3; ++a) dma4<1024>(sb + a * 4096, v2d, rs2, so + a * 128 * 1280);
    };
    const int ir = min(lw * 36 + min(lane, 35), isattn ? 143 : 127);
    int stok;
    {
      int j = ir - 16, t0 = -16 + 2 * (ir & 7) + (ir >> 3), t1 = (j & ~31) + 2 * (j & 15) + ((j >> 4) & 1);
      PIN1(t0); PIN1(t1);
      stok = max(P0 + (isattn ? (ir < 16 ? t0 : t1) : ir), 0);
    }
    const unsigned statoff = WS_STAT + stok * 8;
    unsigned tv; rsrc_t trs;
    {
      const int j = rq * 32 + loc;
      if (lw == 0)      { int L = j * 53 + lane, r = L / 15, c = L - r * 15, s = c / 5; tv = (192 + r) * 3840 + s * 640 + c * 128 + half * 640; trs = mkrs(wqkv, 640 * 3840); if (lane >= 53) tv = OOB; }
      else if (lw == 1) { int L = j * 42 + lane, r = L / 12, c = L - r * 12; tv = (192 + r) * 3072 + c * 128 + half * 1536; trs = mkrs(w1, 640 * 3072); if (lane >= 42) tv = OOB; }
      else              { int n = lw == 2 ? 60 : 25, L = j * n + lane, r = L / 5, c = L - r * 5; tv = r * 1280 + c * 128 + half * 640; trs = lw == 2 ? rs2 : rso; if (lane >= n) tv = OOB; }
    }
    if (ext) waitv<5>(); else waitv<4>();
    STAMP(8);
    unsigned sink;
#pragma nounroll
    for (int t = 0; t < 10; ++t) {
      if (!(ABL & 8) || t == 0) bar();
      if (t == 5) STAMP(6);
      if (t == 6) STAMP(9);
      u2 gr;
      if (t == 7) asm volatile("buffer_load_dwordx2 %0, %1, %2, 0 offen sc1" : "=v"(gr) : "v"(statoff), "s"(rc) : "memory");
      if (t == 0) issue1b(1);
      if (t < 8) {
        issue1a(t + 2); issue1b(t + 2);
        if (ext) waitv<13>(); else if (isattn) waitv<12>(); else waitv<10>();
        if (TOUCH && t == 0) asm volatile("buffer_load_dword %0, %1, %2, 0 offen" : "=v"(sink) : "v"(tv), "s"(trs) : "memory");
      } else {
        issue2(t - 8);
        if (t == 8) waitv<12>();
      }
      if (t == 7) {
        PIN1(gr);
        const unsigned tg = *(LAS unsigned*)(lds + AUX_TAG);
#pragma nounroll
        for (int it = 0; it < (1 << 16) && !__all(gr[1] == tg); ++it) { __builtin_amdgcn_s_sleep(2); gr = ld_ctl(rc, statoff); }
        float r = __builtin_amdgcn_rcpf(hi16(gr[0]));
        *(LAS float*)(lds + AUX_R + ir * 4) = r;
        *(LAS float*)(lds + AUX_NM + ir * 4) = -lo16(gr[0]) * r;
      }
      if (t == 5) STAMP(7);
    }
    STAMP(1);
    bar();
    __builtin_amdgcn_s_setprio(PRIO2);
    asm volatile("" :: "v"(sink));
    const unsigned tlo = rfl(*(LAS unsigned*)(lds + AUX_TAG)), thi = rfl(*(LAS unsigned*)(lds + AUX_TAG + 4));
    u4 W[3][12];
    auto fetch = [&](int G, u4 (&d)[12]) INL {
      const bool isO = G >= 4;
      const unsigned so = isO ? (gphys(G) - 4) * 320 * 1280 : gphys(G) * 384 * 1280;
#pragma unroll
      for (int ks = 0; ks < 12; ++ks)
        d[ks] = __builtin_amdgcn_raw_buffer_load_b128(isO ? rso : rs2, isO ? (ks % 3 == 2 ? v2o3 : v2o) : v2h, so + ((ks / 3) * (isO ? 80 : 96) + (ks % 3) * 32) * 1280, 0);
      SCHED();
    };
    auto put = [&](int slot, u4 (&d)[12]) INL {
      if (ABL & 128) { for (int ks = 0; ks < 12; ++ks) PIN1(d[ks]); return; }
      LAS char* p = lds + slot * SLOT + lw * 12288 + lane * 16;
#pragma unroll
      for (int ks = 0; ks < 12; ++ks) *(LAS u4*)(p + ks * 1024) = d[ks];
    };
    if (isattn) {
      bar();
      if (lw == 0) { poll(rc, myflag1(), tlo, thi); *(LAS unsigned*)(lds + AUX_RDY) = 1; }
      if (SPLITPOLL && lw == 1) poll(rc, myflag(), tlo, thi);
      fetch(2, W[0]); fetch(3, W[1]); fetch(4, W[2]);
      STAMP(2);
      waitv<36>();
      bar();
#pragma unroll
      for (int i = 0; i < 2; ++i) {
        const int tl = 2 * lw + i;
        LAS char* op = lds + SC_O + (16 * tl + c16) * OPITCH + 16 * g;
        const rsrc_t rs = mkrs(actp + (size_t)(panel * 8 + tl) * ACT_TB + (48 + 3 * grp) * 1024, 2560);
        st128<true>(*(LAS u4*)op, rs, lane * 16, 0);
        st128<true>(*(LAS u4*)(op + 64), rs, lane * 16, 1024);
        st128<true>(*(LAS u4*)(op + (g < 2 ? 128 : 0)), rs, lane * 16 + 2048, 0);
        SCHED();
      }
      bar();
      put(0, W[0]);
      waitv<0>();
      if (arrive(ldsb + AUX_CNT) == 3) st_ctl(rc, lane, WS_FLAG + panel * 256 + 128 + grp * 8, tlo, thi);
      STAMP(4);
      fetch(5, W[0]);
      bar();
      put(1, W[1]); put(2, W[2]);
      if (lw == 0) poll(rc, myflag() + 128, tlo, thi);
      bar();
      bar();
      put(1, W[0]);
      bar();
      if (b == 0 && lw == 1) {
#pragma nounroll
        for (int i = 0; i < 4; ++i) poll(rc, WS_FLAG + i * 1024 + g * 256 + (lane & 8) * 16 + (lane & 7) * 8, tlo, thi);
        st_ctl(rc, lane, 0, tlo, thi);
      }
    } else {
      issue2(2);
      bar();
      fetch(3, W[0]);
      if (NF0 > 1) fetch(4, W[1]);
      STAMP(2);
      if (NF0 > 1) waitv<24>(); else waitv<12>();
      bar();
      if (SPLITPOLL && lw == 1) poll(rc, myflag(), tlo, thi);
      if (NF0 <= 1) fetch(4, W[1]);
      bar();
      put(1, W[0]);
      fetch(5, W[2]);
      bar();
      put(2, W[1]);
      if (lw == 0) poll(rc, myflag() + 128, tlo, thi);
      bar();
      bar();
      put(1, W[2]);
      bar();
    }
    STAMP(3);
    STAMP_FLUSH();
    return;
  }

  const bf8 one = ones8();
  unsigned tlo, thi;
  {
    const int row = P0 + 8 * (grp + (isattn ? 0 : 8)) + w;
    const rsrc_t rs = mkrs(x + (size_t)row * 640, 1280);
    bf8 va = asb(__builtin_amdgcn_raw_buffer_load_b128(rs, lane * 16, 0, 0));
    bf8 vb = asb(__builtin_amdgcn_raw_buffer_load_b128(rs, lane * 16 + 1024, 0, 0));
    u2 ep = ld_ctl(rc, 0);
    if (w == 1) *(LAS bf8*)(lds + AUX_ONES + lane * 16) = one;
    if (w == 2) {
      f4 mc, mp; u4 bm;
#pragma unroll
      for (int j = 0; j < 4; ++j) {
        int d = c16 - 4 * g - j;
        mc[j] = (unsigned)d < 8u ? 0.f : -1e30f;
        mp[j] = d < -8 ? 0.f : -1e30f;
      }
#pragma unroll
      for (int j = 0; j < 2; ++j) {
        int key = 4 * g + 2 * j;
        bm[j] = (key <= c16 ? 0xffffu : 0u) | (key + 1 <= c16 ? 0xffff0000u : 0u);
        bm[2 + j] = ~bm[j];
      }
      LAS char* p = lds + AUX_MASK + lane * 16;
      *(LAS f4*)p = mc; *(LAS f4*)(p + 1024) = mp; *(LAS f4*)(p + 2048) = f4{-1e30f, -1e30f, -1e30f, -1e30f}; *(LAS u4*)(p + 3072) = bm;
    }
    float s1 = dot8(vb, one, dot8(va, one, 0.f)), s2 = dot8(vb, vb, dot8(va, va, 0.f));
    auto u = SWAP16(asu(s1), asu(s2));
    float v = asf(u[0]) + asf(u[1]);
    v = dpp_add<0xB1>(v); v = dpp_add<0x4E>(v); v = dpp_add<0x141>(v); v = dpp_add<0x140>(v);
    float mean = (asf(__builtin_amdgcn_readlane(asu(v), 0)) + asf(__builtin_amdgcn_readlane(asu(v), 32))) * (1.f / 640.f);
    float sq = (asf(__builtin_amdgcn_readlane(asu(v), 16)) + asf(__builtin_amdgcn_readlane(asu(v), 48))) * (1.f / 640.f);
    float var = fmx(sq - mean * mean, 0.f);
    unsigned pkd = pk2(mean, __builtin_amdgcn_sqrtf(bfr(bfr(var) + 1e-5f)));
    tlo = rfl(ep[0]) + 1; thi = rfl(ep[1]);
    asm volatile("" :: "v"(cpf));
    st_ctl(rc, lane, WS_STAT + row * 8, pkd, tlo);
    if (w == 0) *(LAS u4*)(lds + AUX_TAG) = u4{tlo, thi, 0, 0};
  }
  const int nd = RB >= 4 ? (RB == 8 ? 8 : 4) : 0;
  const bool dbl = w < nd, idle = w >= 2 * RB - nd;
  const int part = w & 1;
  const int tb = tb0 + (dbl ? (w & ~1) : nd + ((w - nd) >> 1));
  const rsrc_t rsa = mkrs(actp + (size_t)tb * ACT_TB, idle ? 0 : ACT_TB), rsb = mkrs(actp + (size_t)(tb + 1) * ACT_TB, dbl ? ACT_TB : 0);
  u4 R[2][6];
  u2 bias2[2];
  auto xoff = [&](int ln) INL { return (unsigned)((tb * 16 + (ln & 15)) * 640 + ct * 64 + part * 32 + (ln & 16) + (ln >> 5) * 8) * 2; };
  auto first_acts = [&]() INL {
    if (idle) return;
    int ln = lane; PIN1(ln);
    const rsrc_t rb2 = mkrs(b2 + ct * 64 + part * 32, 64);
    bias2[0] = __builtin_amdgcn_raw_buffer_load_b64(rb2, (ln >> 4) * 8, 0, 0);
    bias2[1] = __builtin_amdgcn_raw_buffer_load_b64(rb2, (ln >> 4) * 8, 32, 0);
    SCHED();
    const unsigned va = ln * 16 + part * 6144 + gphys(0) * 12288;
    if (dbl) {
#pragma unroll
      for (int ks = 0; ks < 6; ++ks) {
        R[0][ks] = __builtin_amdgcn_raw_buffer_load_b128(rsa, va, ks * 1024, 16);
        R[1][ks] = __builtin_amdgcn_raw_buffer_load_b128(rsb, va, ks * 1024, 16);
        SCHED();
      }
    } else {
#pragma unroll
      for (int ks = 0; ks < 6; ++ks) { R[0][ks] = __builtin_amdgcn_raw_buffer_load_b128(rsa, va, ks * 1024, 16); SCHED(); }
    }
  };

  if (isattn) {
    const int tau = w >> 2, h = w & 3;
    const int hrow = c16 ^ (tau ? 0 : 8);
    auto xaddr = [&](int r) INL { return (r >> 3) * 1024 + (r & 7) * 64 + ((g ^ ((-(r >> 2)) & 3)) << 4); };
    int sg[3];
#pragma unroll
    for (int j = 0; j < 3; ++j) sg[j] = (j + h) % 3;
    const int wrow = ldsb + (16 * (g >> 1) + 4 * (g & 1) + q4) * 480;
    const int hm = c16 != 0;
    int px = ldsb + XOFF_A + 2048 + tau * 2048 + xaddr(c16), pxo = px + h * 4096;
    int ph = ldsb + (hm ? XOFF_A + xaddr(hrow) : AUX_ONES + (xaddr(hrow) & 255));
    int pw = wrow + 40 * h + 8 * p4, pr[3];
#pragma unroll
    for (int j = 0; j < 3; ++j) pr[j] = wrow + 40 * p4 + 32 + sg[j] * 160;
    f4 Q[4], K[4], V[4], Qh, Kh, Vh, R5[3], X5;
#pragma unroll
    for (int i = 0; i < 4; ++i) { Q[i] = f4{0, 0, 0, 0}; K[i] = Q[i]; V[i] = Q[i]; }
    Qh = Kh = Vh = R5[0] = R5[1] = R5[2] = X5 = Q[0];
    bf8 xf[4], xh, xo, wq, wk, wv, w5[3];
    bar();
    STAMP(14);
    if (w >= 4) __builtin_amdgcn_s_setprio(PRIOY);
    __builtin_amdgcn_s_waitcnt(0xC07F);
#pragma unroll
    for (int j = 0; j < 3; ++j) w5[j] = ldw(LP(pr[j]), 3840);
    xo = ldx(LP(pxo)); SCHED();
    wq = ldw(LP(pw), 3840); SCHED();
    xf[0] = ldx(LP(px)); SCHED();
    wk = ldw(LP(pw) + 160, 3840); SCHED();
#pragma unroll
    for (int i = 1; i < 4; ++i) { xf[i] = ldx(LP(px) + i * 4096); SCHED(); }
    xh = ldx(LP(ph)); SCHED();
    wv = ldw(LP(pw) + 320, 3840);
#pragma nounroll
    for (int s = 0; s < 10; ++s) {
      const int dsl = (s % 3 == 2) ? -2 * SLOT : SLOT;
#pragma unroll
      for (int ks = 0; ks < 2; ++ks) {
        constexpr int dummy = 0; (void)dummy;
        const int ox = ks ? 0 : 512, ow = ks ? 0 : 15360;
        SCHED();
        R5[0] = mfma(w5[0], xo, R5[0]); PIN1(R5[0]); if (ks) { pr[0] += dsl; pr[1] += dsl; PIN1(pr[0]); PIN1(pr[1]); } SCHED();
        R5[1] = mfma(w5[1], xo, R5[1]); PIN1(R5[1]); if (ks) { pr[2] += dsl; pxo += dsl; PIN1(pr[2]); PIN1(pxo); } SCHED();
        R5[2] = mfma(w5[2], xo, R5[2]); PIN1(R5[2]); if (ks) { pw += dsl; px += dsl; PIN1(pw); PIN1(px); } SCHED();
        Q[0] = mfma(wq, xf[0], Q[0]); PIN1(Q[0]); if (ks) { ph += dsl * hm; PIN1(ph); } SCHED();
#pragma unroll
        for (int i = 1; i < 4; ++i) { Q[i] = mfma(wq, xf[i], Q[i]); PIN1(Q[i]); SCHED(); }
#if !(ABL & 2)
        asm volatile("s_cmp_lg_u32 %3, %4\n\ts_cbranch_scc1 .Lqh%=\n\tv_mfma_f32_16x16x32_bf16 %0, %1, %2, %0\n.Lqh%=:"
                     : "+v"(Qh) : "v"(wq), "v"(xh), "s"(tau), "i"(ks) : "scc");
#endif
        X5 = mfma(w5[0], xh, X5); PIN1(X5);
        SCHED();
        if (ks == 1) {
          if (s == 9) {
            int ln = lane; PIN1(ln);
            int a0 = AUX_CS + tau * 960 + 80 * h + (ln & 48), a1 = AUX_CS + sg[0] * 320 + 64 + 5 * (ln & 48), dm = AUX_DUMMY + ln * 16;
            a0 = (ln & 15) ? dm : a0; a1 = (ln & 15) ? dm : a1;
            PIN1(a0); PIN1(a1);
            asm volatile("s_nop 7" : "+v"(Qh));
            *(LAS f4*)(lds + a0) = Qh; *(LAS f4*)(lds + a1) = X5;
          }
          if (!(ABL & 8) || s == 9) bar();
        }
        w5[0] = ldw(LP(pr[0]) + ow, 3840); K[0] = mfma(wk, xf[0], K[0]); PIN1(K[0]); SCHED();
        w5[1] = ldw(LP(pr[1]) + ow, 3840); K[1] = mfma(wk, xf[1], K[1]); PIN1(K[1]); SCHED();
        w5[2] = ldw(LP(pr[2]) + ow, 3840); K[2] = mfma(wk, xf[2], K[2]); PIN1(K[2]); SCHED();
        xo = ldx(LP(pxo) + ox);            K[3] = mfma(wk, xf[3], K[3]); PIN1(K[3]); SCHED();
        wq = ldw(LP(pw) + ow, 3840);       Kh = mfma(wk, xh, Kh); PIN1(Kh); SCHED();
        V[0] = mfma(xf[0], wv, V[0]); PIN1(V[0]); SCHED();
        xf[0] = ldx(LP(px) + ox); wk = ldw(LP(pw) + ow + 160, 3840);
        V[1] = mfma(xf[1], wv, V[1]); PIN1(V[1]); SCHED();
        xf[1] = ldx(LP(px) + ox + 4096);
        V[2] = mfma(xf[2], wv, V[2]); PIN1(V[2]); SCHED();
        xf[2] = ldx(LP(px) + ox + 8192);
        V[3] = mfma(xf[3], wv, V[3]); PIN1(V[3]); SCHED();
        xf[3] = ldx(LP(px) + ox + 12288);
        Vh = mfma(xh, wv, Vh); PIN1(Vh); SCHED();
        xh = ldx(LP(ph) + ox); wv = ldw(LP(pw) + ow + 320, 3840);
        SCHED();
      }
    }
    STAMP(1);
    int ln = lane; PIN1(ln);
    const int cc = ln & 15, g4 = (ln >> 4) * 4;
    LAS char* cb = lds + AUX_CS;
    LAS char* rb = lds + SC_REM + tau * 7680 + cc * 32 + g4 * 2;
    u2 Qp[4], Kp[5], Vp[5];
    f4 nm4[4], r4[4], nmh4, rh4;
    float csv;
    {
      float nm[4], rr[4];
      LAS char* sl = lds + AUX_NM + (16 + 16 * tau + cc) * 4;
      f4 c5[3];
      const float nmo = *(LAS float*)(sl + h * 128), ro = *(LAS float*)(sl + 576 + h * 128);
#pragma unroll
      for (int j = 0; j < 3; ++j) c5[j] = *(LAS f4*)(cb + sg[j] * 320 + 64 + 20 * g4);
      const int hr = cc ^ (tau ? 0 : 8);
      const float nmh = *(LAS float*)(lds + AUX_NM + hr * 4), rh = *(LAS float*)(lds + AUX_R + hr * 4);
#pragma unroll
      for (int i = 0; i < 4; ++i) { nm[i] = *(LAS float*)(sl + i * 128); rr[i] = *(LAS float*)(sl + 576 + i * 128); }
      f4 csq = *(LAS f4*)(cb + 80 * h + 4 * g4), csq2 = *(LAS f4*)(cb + 960 + 80 * h + 4 * g4);
      csv = asf(__builtin_amdgcn_ds_bpermute(cc * 4, asu(Vh[0])));
      f4 csk;
#pragma unroll
      for (int j = 0; j < 4; ++j) csk[j] = asf(__builtin_amdgcn_update_dpp(0u, asu(Kh[j]), 0x150, 0xf, 0xf, true));
      SCHED();
#pragma unroll
      for (int j = 0; j < 3; ++j) *(LAS u2*)(rb + sg[j] * 2560 + (h + 1) * 512) = pk4(R5[j] * ro + c5[j] * nmo);
      *(LAS u2*)(rb + sg[0] * 2560) = pk4(X5 * rh + c5[0] * nmh);
      SCHED();
      Kp[0] = pk4(Kh * rh + csk * nmh);
#pragma unroll
      for (int i = 0; i < 4; ++i) Kp[i + 1] = pk4(K[i] * rr[i] + csk * nm[i]);
      PIN4(Kp[1], Kp[2], Kp[3], Kp[4]); PIN1(Kp[0]);
      SCHED();
      LAS char* sr = lds + AUX_NM + (16 + 16 * tau + g4) * 4;
      const int h4 = g4 ^ (tau ? 0 : 8);
      nmh4 = *(LAS f4*)(lds + AUX_NM + h4 * 4); rh4 = *(LAS f4*)(lds + AUX_R + h4 * 4);
#pragma unroll
      for (int i = 0; i < 4; ++i) { nm4[i] = *(LAS f4*)(sr + i * 128); r4[i] = *(LAS f4*)(sr + 576 + i * 128); }
      SCHED();
      csq += csq2;
#pragma unroll
      for (int i = 0; i < 4; ++i) Qp[i] = pk4(Q[i] * rr[i] + csq * nm[i]);
      PIN4(Qp[0], Qp[1], Qp[2], Qp[3]);
    }
    STAMP(4);
    bar();
    unsigned early;
    {
      LAS char* rt = lds + SC_REM + tau * 7680 + 5120 + ln * 8;
      LAS char* mk = lds + AUX_MASK + ln * 16;
      u2 K5p[5], V5p[5], q5[4];
#pragma unroll
      for (int i = 0; i < 5; ++i) {
        K5p[i] = *(volatile LAS u2*)(rb + 2560 + i * 512);
        if (i < 4) q5[i] = *(LAS u2*)(rb + (i + 1) * 512);
      }
      const f4 mcur = *(LAS f4*)mk, mprev = *(LAS f4*)(mk + 1024), mprev0 = *(LAS f4*)(mk + (panel == 0 ? 2048 : 1024));
      const u4 bm = *(LAS u4*)(mk + 3072);
#pragma unroll
      for (int i = 0; i < 5; ++i) V5p[i] = __builtin_bit_cast(u2, ldt(rt + i * 512));
      SCHED();
      Vp[0] = pk4(Vh * rh4 + nmh4 * csv);
#pragma unroll
      for (int i = 0; i < 4; ++i) Vp[i + 1] = pk4(V[i] * r4[i] + nm4[i] * csv);
      PIN4(Vp[1], Vp[2], Vp[3], Vp[4]); PIN1(Vp[0]);
      SCHED();
      unsigned qm = g4 == 4 * h ? ~0u : 0u;
      PIN1(qm);
      float cexp = 0.22360679774997896f * 1.4426950408889634f;
      PIN1(cexp);
      f4 sv[4];
      float mx[4];
      {
        f4 sc[4], sp[4];
#pragma unroll
        for (int i = 0; i < 4; ++i) {
          bf8 qb = cat(Qp[i], q5[i] & qm);
          sc[i] = mfma(cat(Kp[i + 1], K5p[i + 1]), qb, mcur);
          sp[i] = mfma(cat(Kp[i], K5p[i]), qb, i == 0 ? mprev0 : mprev);
        }
#pragma unroll
        for (int i = 0; i < 4; ++i) {
#pragma unroll
          for (int j = 0; j < 4; ++j) sv[i][j] = fmx(sc[i][j], sp[i][j]);
          mx[i] = fmx(fmx(sv[i][0], sv[i][1]), fmx(sv[i][2], sv[i][3]));
        }
      }
      {
        auto u = SWAP16(asu(mx[0]), asu(mx[1])), v = SWAP16(asu(mx[2]), asu(mx[3]));
        auto t = SWAP32(asu(fmx(asf(u[0]), asf(u[1]))), asu(fmx(asf(v[0]), asf(v[1]))));
        unsigned m = asu(fmx(asf(t[0]), asf(t[1])) * cexp);
        auto a = SWAP32(m, m);
        auto a0 = SWAP16(a[0], a[0]), a1 = SWAP16(a[1], a[1]);
        mx[0] = asf(a0[0]); mx[1] = asf(a0[1]); mx[2] = asf(a1[0]); mx[3] = asf(a1[1]);
      }
      bf8 pb[4];
#pragma unroll
      for (int i = 0; i < 4; ++i) {
        f4 pv;
#pragma unroll
        for (int j = 0; j < 4; ++j) pv[j] = __builtin_amdgcn_exp2f(__builtin_fmaf(sv[i][j], cexp, -mx[i]));
        u2 pp = pk4(pv);
        pb[i] = asb(u4{pp[0], pp[1], pp[0], pp[1]} & bm);
      }
      PIN4(pb[0], pb[1], pb[2], pb[3]);
      STAMP(6);
      early = rfl(*(volatile LAS unsigned*)(lds + AUX_RDY));
      if (early) first_acts();
      const int ob = SC_O + (2 * cc + tau) * OPITCH + 40 * h;
      int ob5 = g4 == 4 * h ? ob + 32 : SC_O + (2 * cc + tau) * OPITCH + 160;
      PIN1(ob5);
      const f4 zero = {0, 0, 0, 0};
#pragma unroll
      for (int i = 0; i < 4; ++i) {
        f4 l = mfma(one, pb[i], zero);
        f4 o = mfma(cat(Vp[i + 1], Vp[i]), pb[i], zero);
        f4 o5 = mfma(cat(V5p[i + 1], V5p[i]), pb[i], zero);
        float inv = __builtin_amdgcn_rcpf(l[0]);
        *(LAS u2*)(lds + ob + i * 32 * OPITCH + 2 * g4) = pk4(o * inv);
        *(LAS u2*)(lds + ob5 + i * 32 * OPITCH) = pk4(o5 * inv);
      }
    }
    STAMP(5);
    bar();
    if (!early) first_acts();
  } else {
    const int tq = w & 3, fh = w >> 2;
    const int rho = 4 * (g & 1) + q4;
    const int role = (tq + 2 * fh) & 3;
    int px = ldsb + XOFF_F + tq * 4096 + (c16 >> 3) * 1024 + (c16 & 7) * 64 + ((g ^ ((-(c16 >> 2)) & 3)) << 4);
    const int wl = (g >> 1) * 12288 + rho * 128 + (((p4 >> 1) ^ (rho & 6)) << 4) + (p4 & 1) * 8;
    int wb[6], pu[3];
    u2 bias[6];
#pragma unroll
    for (int u = 0; u < 3; ++u) {
      pu[u] = 3 * fh + (u + role) % 3;
      wb[2 * u] = ldsb + (wl ^ ((pu[u] & 1) << 6)) + (pu[u] >> 1) * 1024;
      wb[2 * u + 1] = ldsb + (wl ^ ((pu[u] & 1) << 6) ^ 32) + (pu[u] >> 1) * 1024;
      bias[2 * u] = *(const u2*)(b1 + grp * 192 + pu[u] * 32 + 4 * g);
      bias[2 * u + 1] = *(const u2*)(b1 + grp * 192 + pu[u] * 32 + 16 + 4 * g);
    }
    f4 acc[2][6], CS[2];
#pragma unroll
    for (int e = 0; e < 6; ++e) { acc[0][e] = f4{0, 0, 0, 0}; acc[1][e] = acc[0][e]; }
    CS[0] = CS[1] = acc[0][0];
    bf8 onev = one;
    PIN1(onev);
    bf8 xf[2][2], wf[2][6];
    bar();
    STAMP(14);
    if (w >= 4) __builtin_amdgcn_s_setprio(PRIOY);
    __builtin_amdgcn_s_waitcnt(0xC07F);
    xf[0][0] = ldx(LP(px)); xf[0][1] = ldx(LP(px) + 2048); SCHED();
#pragma unroll
    for (int e = 0; e < 6; ++e) { wf[0][e] = ldw(LP(wb[e]), 6144); SCHED(); }
#pragma nounroll
    for (int s = 0; s < 10; ++s) {
      const int dsl = (s % 3 == 2) ? -2 * SLOT : SLOT;
#pragma unroll
      for (int ks = 0; ks < 2; ++ks) {
        const int lk = ks ^ 1;
        SCHED();
#if !(ABL & 2)
        asm volatile("s_cmp_eq_u32 %5, 3\n\ts_cbranch_scc1 .Lcs%=\n\tv_mfma_f32_16x16x32_bf16 %0, %2, %4, %0\n\tv_mfma_f32_16x16x32_bf16 %1, %3, %4, %1\n.Lcs%=:"
                     : "+v"(CS[0]), "+v"(CS[1]) : "v"(wf[ks][0]), "v"(wf[ks][1]), "v"(onev), "s"(role) : "scc");
#endif
        SCHED();
        if (ks == 1) {
#pragma unroll
          for (int e = 0; e < 2; ++e) {
            acc[0][e] = mfma(wf[1][e], xf[1][0], acc[0][e]); PIN1(acc[0][e]);
            wb[3 * e] += dsl; wb[3 * e + 1] += dsl; PIN1(wb[3 * e]); PIN1(wb[3 * e + 1]); SCHED();
            acc[1][e] = mfma(wf[1][e], xf[1][1], acc[1][e]); PIN1(acc[1][e]);
            wb[3 * e + 2] += dsl; PIN1(wb[3 * e + 2]); if (e == 0) { px += dsl; PIN1(px); } SCHED();
          }
          if (s == 9) {
            int ln = lane; PIN1(ln);
            int a0 = AUX_CS + pu[0] * 128 + (ln & 48), dm = AUX_DUMMY + ln * 16;
            a0 = ((ln & 15) || role == 3) ? dm : a0;
            PIN1(a0);
            asm volatile("s_nop 7" : "+v"(CS[0]), "+v"(CS[1]));
            *(LAS f4*)(lds + a0) = CS[0]; *(LAS f4*)(lds + a0 + 64) = CS[1];
          }
          if (!(ABL & 8) || s == 9) bar();
        }
        constexpr int dummy = 0; (void)dummy;
        const int e0 = ks ? 2 : 0;
#pragma unroll
        for (int m = 2 * e0; m < 12; ++m) {
          const int n = m - 2 * e0;
          if (n == 0) { xf[lk][0] = ldx(LP(px) + lk * 512); xf[lk][1] = ldx(LP(px) + lk * 512 + 2048); }
          else if (n <= 6) wf[lk][n - 1] = ldw(LP(wb[n - 1]) + lk * 3072, 6144);
          acc[m & 1][m >> 1] = mfma(wf[ks][m >> 1], xf[ks][m & 1], acc[m & 1][m >> 1]); PIN1(acc[m & 1][m >> 1]); SCHED();
        }
      }
    }
    STAMP(1);
    float nm[2], rr[2];
    f4 cb4[6];
#pragma unroll
    for (int i = 0; i < 2; ++i) { nm[i] = *(LAS float*)(lds + AUX_NM + (32 * tq + 16 * i + c16) * 4); rr[i] = *(LAS float*)(lds + AUX_R + (32 * tq + 16 * i + c16) * 4); }
#pragma unroll
    for (int e = 0; e < 6; ++e) cb4[e] = *(LAS f4*)(lds + AUX_CS + (pu[e >> 1] * 32 + (e & 1) * 16 + 4 * g) * 4);
#pragma unroll
    for (int i = 0; i < 2; ++i) {
      const rsrc_t rs = mkrs(actp + (size_t)(panel * 8 + 2 * tq + i) * ACT_TB + 6 * grp * 1024, 6144);
#pragma unroll
      for (int u = 0; u < 3; ++u) {
        u2 hp[2];
#pragma unroll
        for (int d = 0; d < 2; ++d) {
          const int e = 2 * u + d;
          f4 v = acc[i][e] * rr[i] + (cb4[e] * nm[i] + unpk4(bias[e]));
#pragma unroll
          for (int j = 0; j < 4; ++j) v[j] = v[j] * fmx(v[j], 0.f);
          hp[d] = pk4(v);
        }
        st128<true>(u4{hp[0][0], hp[0][1], hp[1][0], hp[1][1]}, rs, lane * 16, pu[u] * 1024);
        SCHED();
      }
    }
    STAMP(6);
    bar();
    waitv<0>();
    if (arrive(ldsb + AUX_CNT) == 7) st_ctl(rc, lane, WS_FLAG + panel * 256 + grp * 8, tlo, thi);
    STAMP(2);
    if (w == 7) poll(rc, myflag1(), tlo, thi);
    bar();
    first_acts();
  }

  if (idle) {
    bar(); bar(); bar(); bar(); bar();
    STAMP_FLUSH();
    return;
  }
  auto phase2 = [&](auto nrc) INL {
    constexpr int NR = decltype(nrc)::value;
    STAMP(7);
    int ln = lane; PIN1(ln);
    const int rho = (ln >> 2) & 7;
    const int wl = ((ln >> 5) * 24576 + rho * 128 + ((((ln >> 1) & 1) ^ (rho & 6)) << 4) + (ln & 1) * 8 + part * 6144) ^ (part << 6);
    const unsigned va0 = ln * 16 + part * 6144;
    LAS char* xw = lds + w * 4096 + ln * 16;
    LAS char* xd = lds + (w ^ 1) * 4096 + ln * 16;
    bf8 wf[2][4];
#pragma unroll
    for (int c = 0; c < 4; ++c) { wf[0][c] = ldw(lds + SLOT + (wl ^ (c << 5)), 12288); SCHED(); }
    auto group = [&](auto lastc, int G, int slot, int nslot, f4 (&acc)[NR][4]) INL {
      constexpr bool LAST = decltype(lastc)::value;
      const unsigned va = va0 + gphys(G + 1) * 12288;
      const unsigned va3 = (G >= 3 && ln >= 32) ? OOB : va;
      SCHED();
#pragma unroll
      for (int ks = 0; ks < 6; ++ks) {
        if (ks == 5) {
          if (!LAST) bar();
          STAMP(8 + G);
        }
        LAS char* sp = ks == 5 ? lds + nslot : lds + slot + (ks + 1) * 1024;
#pragma unroll
        for (int c = 0; c < 4; ++c) {
          if (!LAST || ks < 5) wf[(ks + 1) & 1][c] = ldw(sp + (wl ^ (c << 5)), 12288);
#pragma unroll
          for (int r = 0; r < NR; ++r) { acc[r][c] = mfma(wf[ks & 1][c], asb(R[r][ks]), acc[r][c]); PIN1(acc[r][c]); }
          SCHED();
        }
        if (!LAST && !(ABL & 64)) {
          R[0][ks] = __builtin_amdgcn_raw_buffer_load_b128(rsa, (ks % 3 == 2) ? va3 : va, ks * 1024, 16);
          if (NR == 2) R[1][ks] = __builtin_amdgcn_raw_buffer_load_b128(rsb, (ks % 3 == 2) ? va3 : va, ks * 1024, 16);
          SCHED();
        }
      }
    };
    f4 F[NR][4], S[NR][4];
#pragma unroll
    for (int r = 0; r < NR; ++r)
#pragma unroll
      for (int c = 0; c < 4; ++c) F[r][c] = f4{0, 0, 0, 0};
    int slot = SLOT;
#pragma nounroll
    for (int G = 0; G < 4; ++G) {
      const int nslot = slot == 2 * SLOT ? 0 : slot + SLOT;
      group(std::false_type{}, G, slot, nslot, F);
      slot = nslot;
    }
#pragma unroll
    for (int r = 0; r < NR; ++r) {
      *(LAS f4*)(xw + r * 2048) = F[r][2]; *(LAS f4*)(xw + r * 2048 + 1024) = F[r][3];
#pragma unroll
      for (int c = 0; c < 4; ++c) S[r][c] = f4{0, 0, 0, 0};
    }
    group(std::false_type{}, 4, 2 * SLOT, SLOT, S);
    PIN1(bias2[0]); PIN1(bias2[1]);
#pragma unroll
    for (int r = 0; r < NR; ++r)
#pragma unroll
      for (int c = 0; c < 2; ++c) {
        f4 f = F[r][c] + *(LAS f4*)(xd + r * 2048 + c * 1024) + unpk4(bias2[c]);
#pragma unroll
        for (int j = 0; j < 4; ++j) F[r][c][j] = f[j] * __builtin_amdgcn_rcpf(1.f + __builtin_amdgcn_exp2f(f[j] * -1.4426950408889634f));
        PIN1(F[r][c]);
      }
    SCHED();
    u4 xr[NR];
#pragma unroll
    for (int r = 0; r < NR; ++r) xr[r] = __builtin_amdgcn_raw_buffer_load_b128(mkrs(x, 2048 * 1280), xoff(ln), r * 16 * 1280, 0);
    group(std::true_type{}, 5, SLOT, SLOT, S);
#pragma unroll
    for (int r = 0; r < NR; ++r) { *(LAS f4*)(xw + 2 * SLOT + r * 2048) = S[r][2]; *(LAS f4*)(xw + 2 * SLOT + r * 2048 + 1024) = S[r][3]; }
    bar();
    const rsrc_t ro = mkrs(out, 2048 * 1280);
#pragma unroll
    for (int r = 0; r < NR; ++r) {
      f4 sa = S[r][0] + *(LAS f4*)(xd + 2 * SLOT + r * 2048), sb = S[r][1] + *(LAS f4*)(xd + 2 * SLOT + r * 2048 + 1024);
      u2 ya = pk4(sa * F[r][0]), yb = pk4(sb * F[r][1]);
      auto t0 = SWAP16(ya[0], yb[0]), t1 = SWAP16(ya[1], yb[1]);
      u4 y = {t0[0], t1[0], t0[1], t1[1]}, o;
      PIN1(xr[r]);
#pragma unroll
      for (int d = 0; d < 4; ++d) o[d] = pk2(lo16(xr[r][d]) + lo16(y[d]), hi16(xr[r][d]) + hi16(y[d]));
      st128<false>(o, ro, xoff(ln), r * 16 * 1280);
    }
    STAMP(3);
    STAMP_FLUSH();
  };
  if (dbl) phase2(std::integral_constant<int, 2>{});
  else phase2(std::integral_constant<int, 1>{});
}

extern "C" void kernel_launch(void* const* d_in, const int* in_sizes, int n_in, void* d_out, int out_size, void* d_ws, size_t ws_size,
                              hipStream_t stream) {
  (void)in_sizes; (void)n_in; (void)out_size; (void)ws_size;
  (void)hipFuncSetAttribute((const void*)fused, hipFuncAttributeMaxDynamicSharedMemorySize, LDS_BYTES);
  fused<<<256, 768, LDS_BYTES, stream>>>((const u16*)d_in[0], (const u16*)d_in[1], (const u16*)d_in[2], (const u16*)d_in[4],
                                         (const u16*)d_in[5], (const u16*)d_in[6], (const u16*)d_in[7], (u16*)d_out, (char*)d_ws);
}
```

```cpp
#include <hip/hip_runtime.h>
#include <stdint.h>
#include <type_traits>

typedef unsigned short u16;
typedef unsigned long long u64;
typedef __bf16 bf8 __attribute__((ext_vector_type(8)));
typedef __bf16 bf4 __attribute__((ext_vector_type(4)));
typedef __bf16 bf2 __attribute__((ext_vector_type(2)));
typedef float f4 __attribute__((ext_vector_type(4)));
typedef float f2 __attribute__((ext_vector_type(2)));
typedef unsigned u4 __attribute__((ext_vector_type(4)));
typedef unsigned u2 __attribute__((ext_vector_type(2)));
typedef __amdgpu_buffer_rsrc_t rsrc_t;
#define LAS __attribute__((address_space(3)))
#define DI __device__ __forceinline__
#define INL __attribute__((always_inline))

#define STAMPS 0
#define ABL 0
#define PRIO 3
#define HOT 0
#define PRIOY 0
#define PRIO2 0
#define SPLITPOLL 1
#define NF0 1
#define LFIRST 1
#define CPF 1
#define RBF 6
#define TOUCH 1
#define ROT 0
#define ROT2 (SPLITPOLL ? 2 * half : 0)

constexpr int SLOT = 49152;
constexpr int AUX = 3 * SLOT;
constexpr int AUX_ONES = AUX;
constexpr int AUX_NM = AUX + 1024;
constexpr int AUX_R = AUX_NM + 576;
constexpr int AUX_CS = AUX_NM + 1152;
constexpr int AUX_TAG = AUX_CS + 1280;
constexpr int AUX_CNT = AUX_TAG + 8;
constexpr int AUX_RDY = AUX_TAG + 12;
constexpr int AUX_DUMMY = AUX_TAG + 16;
constexpr int AUX_MASK = AUX_DUMMY + 1024;
constexpr int AUX_ST = AUX_MASK + 4096;
constexpr int LDS_BYTES = AUX_ST + (STAMPS ? 1536 : 0);
constexpr int XOFF_A = 30720;
constexpr int XOFF_F = 24576;
constexpr int SC_REM = 0;
constexpr int SC_O = 16384;
constexpr int OPITCH = 176;

constexpr int WS_FLAG = 4096;
constexpr int WS_STAT = 16384;
constexpr int WS_STAMP = 65536;
constexpr size_t WS_ACT = 1 << 20;
constexpr int ACT_TB = 72 * 1024;
constexpr int RBA = 10 - RBF;
constexpr unsigned OOB = 0x80000000u;
constexpr unsigned CODE_BYTES = 28672;

DI void bar() { asm volatile("s_waitcnt lgkmcnt(0)\n\ts_barrier" ::: "memory"); }
template <int N> DI void waitv() { asm volatile("s_waitcnt vmcnt(%0)" ::"i"(N) : "memory"); }
DI f4 mfma(bf8 a, bf8 b, f4 c) { return __builtin_amdgcn_mfma_f32_16x16x32_bf16(a, b, c, 0, 0, 0); }
DI unsigned rfl(unsigned v) { return __builtin_amdgcn_readfirstlane(v); }
DI rsrc_t mkrs(const void* p, unsigned bytes) { return __builtin_amdgcn_make_buffer_rsrc((void*)p, 0, bytes, 0x00020000); }
#define DMA_L(V, OFF) "buffer_load_dwordx4 " V ", %[rs], %[so] offen offset:" OFF " lds\n\t"
template <int STEP> DI void dma4(unsigned ldsaddr, const unsigned* v, rsrc_t rs, unsigned soff) {
  if (ABL & 1) return;
  asm volatile("s_mov_b32 m0, %[l]\n\ts_nop 0\n\t" DMA_L("%[a]", "0") DMA_L("%[b]", "%[o1]") DMA_L("%[c]", "%[o2]") DMA_L("%[d]", "%[o3]")
               :: [l] "s"(ldsaddr), [a] "v"(v[0]), [b] "v"(v[1]), [c] "v"(v[2]), [d] "v"(v[3]), [rs] "s"(rs), [so] "s"(soff),
                  [o1] "i"(STEP), [o2] "i"(2 * STEP), [o3] "i"(3 * STEP) : "memory");
}
template <int STEP> DI void dma2(unsigned ldsaddr, const unsigned* v, rsrc_t rs, unsigned soff) {
  if (ABL & 1) return;
  asm volatile("s_mov_b32 m0, %[l]\n\ts_nop 0\n\t" DMA_L("%[a]", "0") DMA_L("%[b]", "%[o1]")
               :: [l] "s"(ldsaddr), [a] "v"(v[0]), [b] "v"(v[1]), [rs] "s"(rs), [so] "s"(soff), [o1] "i"(STEP) : "memory");
}
DI void dma1(unsigned ldsaddr, unsigned v, rsrc_t rs, unsigned soff) {
  if (ABL & 1) return;
  asm volatile("s_mov_b32 m0, %[l]\n\ts_nop 0\n\t" DMA_L("%[a]", "0") :: [l] "s"(ldsaddr), [a] "v"(v), [rs] "s"(rs), [so] "s"(soff) : "memory");
}
DI void dma8x60(unsigned ldsaddr, const unsigned* v, rsrc_t rs, unsigned soff) {
  if (ABL & 1) return;
  asm volatile("s_mov_b32 m0, %[l]\n\ts_mov_b32 exec_hi, 0x0fffffff\n\t" DMA_L("%[a]", "0") DMA_L("%[b]", "960") DMA_L("%[c]", "1920") DMA_L("%[d]", "2880")
               "s_add_u32 m0, m0, 3840\n\ts_nop 0\n\t" DMA_L("%[e]", "0") DMA_L("%[f]", "960") DMA_L("%[g]", "1920") DMA_L("%[h]", "2880") "s_mov_b32 exec_hi, -1"
               :: [l] "s"(ldsaddr), [a] "v"(v[0]), [b] "v"(v[1]), [c] "v"(v[2]), [d] "v"(v[3]), [e] "v"(v[4]), [f] "v"(v[5]), [g] "v"(v[6]), [h] "v"(v[7]),
                  [rs] "s"(rs), [so] "s"(soff) : "memory", "scc");
}
DI rsrc_t mkrs4k(const void* p, unsigned bytes) { return mkrs((const char*)p - 4096, bytes + 4096); }
DI bf8 ldx(LAS char* p) { return *(LAS bf8*)p; }
DI bf4 ldt(LAS char* p) { return __builtin_amdgcn_ds_read_tr16_b64_v4bf16((LAS bf4*)p); }
template <bool SC1> DI void st128(u4 v, rsrc_t rs, unsigned voff, unsigned soff) {
  if (SC1) asm volatile("buffer_store_dwordx4 %0, %1, %2, %3 offen sc1\n\ts_nop 1" :: "v"(v), "v"(voff), "s"(rs), "s"(soff) : "memory");
  else asm volatile("buffer_store_dwordx4 %0, %1, %2, %3 offen\n\ts_nop 1" :: "v"(v), "v"(voff), "s"(rs), "s"(soff) : "memory");
}
DI LAS char* LP(int a) { return (LAS char*)(unsigned)a; }
DI bf8 ldw(LAS char* p, int tstride) { return __builtin_shufflevector(ldt(p), ldt(p + tstride), 0, 1, 2, 3, 4, 5, 6, 7); }
struct WF { bf4 lo, hi; };
DI bf8 fr(const WF& f) { return __builtin_shufflevector(f.lo, f.hi, 0, 1, 2, 3, 4, 5, 6, 7); }
DI float dot8(bf8 a, bf8 b, float c) {
  c = __builtin_amdgcn_fdot2_f32_bf16(__builtin_shufflevector(a, a, 0, 1), __builtin_shufflevector(b, b, 0, 1), c, false);
  c = __builtin_amdgcn_fdot2_f32_bf16(__builtin_shufflevector(a, a, 2, 3), __builtin_shufflevector(b, b, 2, 3), c, false);
  c = __builtin_amdgcn_fdot2_f32_bf16(__builtin_shufflevector(a, a, 4, 5), __builtin_shufflevector(b, b, 4, 5), c, false);
  c = __builtin_amdgcn_fdot2_f32_bf16(__builtin_shufflevector(a, a, 6, 7), __builtin_shufflevector(b, b, 6, 7), c, false);
  return c;
}
DI bf8 asb(u4 v) { return __builtin_bit_cast(bf8, v); }
DI bf8 ones8() { return asb(u4{0x3f803f80u, 0x3f803f80u, 0x3f803f80u, 0x3f803f80u}); }
DI float asf(unsigned u) { return __builtin_bit_cast(float, u); }
DI unsigned asu(float f) { return __builtin_bit_cast(unsigned, f); }
DI unsigned pk2(float a, float b) { f2 v = {a, b}; return __builtin_bit_cast(unsigned, __builtin_convertvector(v, bf2)); }
DI u2 pk4(f4 v) { u2 r = {pk2(v[0], v[1]), pk2(v[2], v[3])}; return r; }
DI float bfr(float a) { return asf(pk2(a, 0.f) << 16); }
DI float lo16(unsigned u) { return asf(u << 16); }
DI float hi16(unsigned u) { return asf(u & 0xffff0000u); }
DI f4 unpk4(u2 v) { return f4{lo16(v[0]), hi16(v[0]), lo16(v[1]), hi16(v[1])}; }
DI bf8 cat(u2 a, u2 b) { return asb(u4{a[0], a[1], b[0], b[1]}); }
DI float fmx(float a, float b) { return __builtin_elementwise_maximum(a, b); }
template <int CTRL> DI float dpp_add(float v) { return v + asf(__builtin_amdgcn_update_dpp(0u, asu(v), CTRL, 0xf, 0xf, true)); }
#define SWAP16(a, b) __builtin_amdgcn_permlane16_swap(a, b, false, false)
#define SWAP32(a, b) __builtin_amdgcn_permlane32_swap(a, b, false, false)
#define PIN4(a, b, c, d) asm volatile("" : "+v"(a), "+v"(b), "+v"(c), "+v"(d))
#define PIN1(a) asm volatile("" : "+v"(a))
#define SCHED() __builtin_amdgcn_sched_barrier(0)
DI u2 ld_ctl(rsrc_t rc, unsigned off) { asm volatile("" ::: "memory"); return __builtin_amdgcn_raw_buffer_load_b64(rc, off, 0, 16); }
DI void st_ctl(rsrc_t rc, int lane, unsigned off, unsigned lo, unsigned hi) {
  __builtin_amdgcn_raw_buffer_store_b64(u2{lo, hi}, rc, lane == 0 ? off : OOB, 0, 16);
  asm volatile("" ::: "memory");
}
DI void poll(rsrc_t rc, unsigned off, unsigned tlo, unsigned thi) {
#pragma nounroll
  for (int it = 0; it < (1 << 16); ++it) {
    u2 v = ld_ctl(rc, off);
    if (__all(v[0] == tlo && v[1] == thi)) break;
    __builtin_amdgcn_s_sleep(2);
  }
  asm volatile("" ::: "memory");
}
DI unsigned arrive(unsigned ldsaddr) {
  unsigned r, one = 1;
  asm volatile("s_mov_b64 exec, 1\n\tds_add_rtn_u32 %0, %1, %2\n\ts_waitcnt lgkmcnt(0)\n\ts_mov_b64 exec, -1" : "=&v"(r) : "v"(ldsaddr), "v"(one) : "memory");
  return rfl(r);
}
#define STAMP(i)
#define STAMP_INIT()
#define STAMP_FLUSH()

__global__ __launch_bounds__(768, 3) void fused(const u16* __restrict__ x, const u16* __restrict__ wqkv, const u16* __restrict__ wo,
                                                const u16* __restrict__ w1, const u16* __restrict__ b1, const u16* __restrict__ w2,
                                                const u16* __restrict__ b2, u16* __restrict__ out, char* __restrict__ ws) {
  extern __shared__ __attribute__((aligned(16))) char smem[];
  LAS char* lds = (LAS char*)smem;
  const unsigned ldsb = (unsigned)(size_t)lds;
  const int tid = threadIdx.x, lane = tid & 63, w = rfl(tid >> 6);
  unsigned cpf = 0;
  if (CPF) {
    u64 pc;
    asm volatile("s_getpc_b64 %0" : "=s"(pc));
    if (w < 4) cpf = __builtin_amdgcn_raw_buffer_load_b32(mkrs((const void*)pc, CODE_BYTES), tid * 128 + ((blockIdx.x >> 3) & 3 ? OOB : 0), 0, 0);
  }
  const int b = blockIdx.x, xcd = b & 7, loc = b >> 3;
  const int rq = xcd >> 1, half = xcd & 1;
  const bool isattn = loc < 16;
  const int li = loc & 15;
  const int panel = 4 * rq + (li & 3);
  const int grp = 4 * half + (li >> 2);
  const int P0 = panel * 128;
  const int rot = ROT;
  int ctl, rb0;
  if (isattn) { if (li < 6) { ctl = li >> 1; rb0 = 24 + 4 * (li & 1); } else { ctl = 3 + (li - 6) / 5; rb0 = 12 + 4 * ((li - 6) % 5); } }
  else        { if (li < 12) { ctl = li >> 2; rb0 = 6 * (li & 3); } else { ctl = 3 + ((li - 12) >> 1); rb0 = 6 * (li & 1); } }
  const int ct = 5 * half + ctl;
  const int tb0 = rq * 32 + rb0;
  const int RB = isattn ? RBA : RBF;
  const int rot2 = ROT2;
  auto gphys = [&](int G) INL { return G < 4 ? (G + rot2) & 3 : G < 6 ? 4 + ((G + (rot2 >> 1)) & 1) : 6; };
  const int g = lane >> 4, c16 = lane & 15, q4 = (lane >> 2) & 3, p4 = lane & 3;
  const rsrc_t rc = mkrs(ws, 65536);
  auto myflag = [&]() INL { return WS_FLAG + ((lane & 8) ? (tb0 + RB - 1) >> 3 : tb0 >> 3) * 256 + (lane & 7) * 8; };
  auto myflag1 = [&]() INL { return SPLITPOLL ? (myflag() & ~32) | (half << 5) : myflag(); };
  char* actp = ws + WS_ACT;
  STAMP_INIT();
  STAMP(0);

  if (__builtin_expect(w >= 8, LFIRST)) {
    __builtin_amdgcn_s_setprio(PRIO);
    const int lw = w - 8;
    const int rho = lane >> 3, cs8 = lane & 7, hh = lw >> 1, tt = lw & 1;
    const rsrc_t rsx = mkrs4k(x, 2048 * 1280);
    const rsrc_t rsw = isattn ? mkrs4k(wqkv, 640 * 3840) : mkrs4k(w1, 640 * 3072);
    const unsigned swz = (cs8 ^ (rho & 6)) << 4;
    unsigned vx[5], vw[8];
    {
      const int r8 = (lane >> 2) & 7, r2 = r8 >> 2;
      const unsigned col = (lane >> 5) * 64 + 4096;
#pragma unroll
      for (int i = 0; i < 4; ++i) {
        const int row = isattn ? P0 + 32 * lw + 16 * (i & 1) + (i >> 1) + 2 * r8 : P0 + 32 * lw + 8 * i + r8;
        vx[i] = row * 1280 + col + ((p4 ^ ((-(2 * (i & 1) + r2)) & 3)) << 4) - i * 1024;
      }
      vx[4] = max(P0 - 16 + tt + 2 * r8, 0) * 1280 + col + ((p4 ^ ((-(2 * tt + r2)) & 3)) << 4);
    }
    if (isattn) {
      const int r2 = lane >= 30, cc = lane - 30 * r2, seg = (cc >= 10) + (cc >= 20);
      const unsigned base = (16 * lw + r2) * 3840 + seg * 1120 + cc * 16 + grp * 160 + 4096;
#pragma unroll
      for (int i = 0; i < 8; ++i) vw[i] = base + (4 * (i >> 2) + 8 * ((i >> 1) & 1) + 2 * (i & 1)) * 3840 - (i & 3) * 960;
    } else {
      const unsigned base = (16 * hh + 4 * tt + (rho >> 2) * 8 + (rho & 3)) * 3072 + grp * 384 + swz + 4096;
#pragma unroll
      for (int i = 0; i < 6; ++i) vw[i] = base + (i / 3) * 32 * 3072 + (i % 3) * 128 - (i & 3) * 1024;
      vw[6] = vw[7] = 0;
    }
    const bool ext = isattn && lw < 2;
    asm volatile("s_nop 4" ::: "memory");

    auto issue1a = [&](int t) INL {
      int kt = t + rot; kt = kt >= 10 ? kt - 10 : kt; if (HOT) kt = kt & 1;
      const unsigned sb = ldsb + (t % 3) * SLOT;
      if (!(ABL & 16)) dma4<1024>(sb + (isattn ? XOFF_A + 2048 : XOFF_F) + lw * 4096, vx, rsx, kt * 128);
      if (ext) dma1(sb + XOFF_A + lw * 1024, vx[4], rsx, kt * 128);
    };
    auto issue1b = [&](int t) INL {
      int kt = t + rot; kt = kt >= 10 ? kt - 10 : kt; if (HOT) kt = kt & 1;
      const unsigned sb = ldsb + (t % 3) * SLOT;
      if (ABL & 32) return;
      if (isattn) {
        dma8x60(sb + lw * 7680, vw, rsw, kt * (64 * 3840));
      } else {
        dma4<1024>(sb + lw * 6144, vw, rsw, kt * (64 * 3072));
        dma2<1024>(sb + lw * 6144 + 4096, vw + 4, rsw, kt * (64 * 3072));
      }
    };
    STAMP(10);
    issue1a(0); issue1b(0); issue1a(1);
    const rsrc_t rs2 = mkrs(w2, 1536 * 1280), rso = mkrs(wo, 640 * 1280);
    const unsigned v2h = (16 * tt + 8 * hh + rho) * 1280 + ct * 128 + swz;
    const unsigned v2o = (16 * hh + 8 * (rho >> 2) + 4 * tt + (rho & 3)) * 1280 + ct * 128 + swz;
    const unsigned v2o3 = hh ? OOB : v2o;
    unsigned v2d[4];
#pragma unroll
    for (int i = 0; i < 4; ++i) v2d[i] = v2h + i * (32 * 1280 - 1024);
    auto issue2 = [&](int G) INL {
      const unsigned sb = ldsb + ((G + 1) % 3) * SLOT + lw * 12288, so = gphys(G) * 384 * 1280;
#pragma unroll
      for (int a = 0; a < 3; ++a) dma4<1024>(sb + a * 4096, v2d, rs2, so + a * 128 * 1280);
    };
    const int ir = min(lw * 36 + min(lane, 35), isattn ? 143 : 127);
    int stok;
    {
      int j = ir - 16, t0 = -16 + 2 * (ir & 7) + (ir >> 3), t1 = (j & ~31) + 2 * (j & 15) + ((j >> 4) & 1);
      PIN1(t0); PIN1(t1);
      stok = max(P0 + (isattn ? (ir < 16 ? t0 : t1) : ir), 0);
    }
    const unsigned statoff = WS_STAT + stok * 8;
    unsigned tv; rsrc_t trs;
    {
      const int j = rq * 32 + loc;
      if (lw == 0)      { int L = j * 53 + lane, r = L / 15, c = L - r * 15, s = c / 5; tv = (192 + r) * 3840 + s * 640 + c * 128 + half * 640; trs = mkrs(wqkv, 640 * 3840); if (lane >= 53) tv = OOB; }
      else if (lw == 1) { int L = j * 42 + lane, r = L / 12, c = L - r * 12; tv = (192 + r) * 3072 + c * 128 + half * 1536; trs = mkrs(w1, 640 * 3072); if (lane >= 42) tv = OOB; }
      else              { int n = lw == 2 ? 60 : 25, L = j * n + lane, r = L / 5, c = L - r * 5; tv = r * 1280 + c * 128 + half * 640; trs = lw == 2 ? rs2 : rso; if (lane >= n) tv = OOB; }
    }
    auto wait1 = [&]() INL { if (ext) waitv<13>(); else if (isattn) waitv<12>(); else waitv<10>(); };
    if (ext) waitv<5>(); else waitv<4>();
    STAMP(8);
    bar();
    const int srow = P0 + 8 * (grp + (isattn ? 0 : 8)) + 2 * lw;
    u4 ra, rb, rcc;
    u2 ep;
    unsigned sink;
    {
      const rsrc_t rs = mkrs(x + (size_t)srow * 640, 2560);
      const unsigned o = (lane >> 5) * 1280 + (lane & 31) * 16, o3 = (lane & 31) < 16 ? o : OOB, z = 0;
      asm volatile("s_nop 4\n\tbuffer_load_dwordx4 %0, %4, %6, 0 offen\n\tbuffer_load_dwordx4 %1, %4, %6, 0 offen offset:512\n\t"
                   "buffer_load_dwordx4 %2, %5, %6, 0 offen offset:1024\n\tbuffer_load_dwordx2 %3, %8, %7, 0 offen sc1"
                   : "=&v"(ra), "=&v"(rb), "=&v"(rcc), "=&v"(ep) : "v"(o), "v"(o3), "s"(rs), "s"(rc), "v"(z) : "memory");
    }
    issue1b(1); issue1a(2); issue1b(2);
    wait1();
    if (TOUCH) asm volatile("buffer_load_dword %0, %1, %2, 0 offen" : "=v"(sink) : "v"(tv), "s"(trs) : "memory");
    if (!(ABL & 8)) bar();
    issue1a(3); issue1b(3);
    PIN4(ra, rb, rcc, ep);
    const unsigned tlo = rfl(ep[0]) + 1, thi = rfl(ep[1]);
    {
      const bf8 one = ones8();
      float s1 = dot8(asb(rcc), one, dot8(asb(rb), one, dot8(asb(ra), one, 0.f)));
      float s2 = dot8(asb(rcc), asb(rcc), dot8(asb(rb), asb(rb), dot8(asb(ra), asb(ra), 0.f)));
      auto u = SWAP16(asu(s1), asu(s2));
      float v = asf(u[0]) + asf(u[1]);
      v = dpp_add<0xB1>(v); v = dpp_add<0x4E>(v); v = dpp_add<0x141>(v); v = dpp_add<0x140>(v);
      auto u2_ = SWAP16(asu(v), asu(v));
      float mean = asf(u2_[0]) * (1.f / 640.f);
      float var = fmx(asf(u2_[1]) * (1.f / 640.f) - mean * mean, 0.f);
      unsigned pkd = pk2(mean, __builtin_amdgcn_sqrtf(bfr(bfr(var) + 1e-5f)));
      const unsigned so = (lane & 31) ? OOB : WS_STAT + (srow + (lane >> 5)) * 8;
      asm volatile("buffer_store_dwordx2 %0, %1, %2, 0 offen sc1" :: "v"(u2{pkd, tlo}), "v"(so), "s"(rc) : "memory");
      if (lw == 0) *(LAS u4*)(lds + AUX_TAG) = u4{tlo, thi, 0, 0};
    }
    if (ext) waitv<14>(); else if (isattn) waitv<13>(); else waitv<11>();
#pragma nounroll
    for (int t = 2; t < 7; ++t) {
      if (!(ABL & 8)) bar();
      if (t == 5) STAMP(6);
      if (t == 6) STAMP(9);
      issue1a(t + 2); issue1b(t + 2);
      wait1();
      if (t == 5) STAMP(7);
    }
    if (!(ABL & 8)) bar();
    u2 gr;
    asm volatile("buffer_load_dwordx2 %0, %1, %2, 0 offen sc1" : "=v"(gr) : "v"(statoff), "s"(rc) : "memory");
    issue1a(9); issue1b(9);
    wait1();
    if (!(ABL & 8)) bar();
    issue2(0);
    PIN1(gr);
#pragma nounroll
    for (int it = 0; it < (1 << 16) && !__all(gr[1] == tlo); ++it) { __builtin_amdgcn_s_sleep(2); gr = ld_ctl(rc, statoff); }
    {
      float r = __builtin_amdgcn_rcpf(hi16(gr[0]));
      *(LAS float*)(lds + AUX_R + ir * 4) = r;
      *(LAS float*)(lds + AUX_NM + ir * 4) = -lo16(gr[0]) * r;
    }
    waitv<12>();
    if (!(ABL & 8)) bar();
    issue2(1);
    STAMP(1);
    bar();
    __builtin_amdgcn_s_setprio(PRIO2);
    asm volatile("" :: "v"(sink));
    u4 W[3][12];
    auto fetch = [&](int G, u4 (&d)[12]) INL {
      const bool isO = G >= 4;
      const unsigned so = isO ? (gphys(G) - 4) * 320 * 1280 : gphys(G) * 384 * 1280;
#pragma unroll
      for (int ks = 0; ks < 12; ++ks)
        d[ks] = __builtin_amdgcn_raw_buffer_load_b128(isO ? rso : rs2, isO ? (ks % 3 == 2 ? v2o3 : v2o) : v2h, so + ((ks / 3) * (isO ? 80 : 96) + (ks % 3) * 32) * 1280, 0);
      SCHED();
    };
    auto put = [&](int slot, u4 (&d)[12]) INL {
      if (ABL & 128) { for (int ks = 0; ks < 12; ++ks) PIN1(d[ks]); return; }
      LAS char* p = lds + slot * SLOT + lw * 12288 + lane * 16;
#pragma unroll
      for (int ks = 0; ks < 12; ++ks) *(LAS u4*)(p + ks * 1024) = d[ks];
    };
    if (isattn) {
      bar();
      if (lw == 0) { poll(rc, myflag1(), tlo, thi); *(LAS unsigned*)(lds + AUX_RDY) = 1; }
      if (SPLITPOLL && lw == 1) poll(rc, myflag(), tlo, thi);
      fetch(2, W[0]); fetch(3, W[1]); fetch(4, W[2]);
      STAMP(2);
      waitv<36>();
      bar();
#pragma unroll
      for (int i = 0; i < 2; ++i) {
        const int tl = 2 * lw + i;
        LAS char* op = lds + SC_O + (16 * tl + c16) * OPITCH + 16 * g;
        const rsrc_t rs = mkrs(actp + (size_t)(panel * 8 + tl) * ACT_TB + (48 + 3 * grp) * 1024, 2560);
        st128<true>(*(LAS u4*)op, rs, lane * 16, 0);
        st128<true>(*(LAS u4*)(op + 64), rs, lane * 16, 1024);
        st128<true>(*(LAS u4*)(op + (g < 2 ? 128 : 0)), rs, lane * 16 + 2048, 0);
        SCHED();
      }
      waitv<0>();
      if (arrive(ldsb + AUX_CNT) == 3) st_ctl(rc, lane, WS_FLAG + panel * 256 + 128 + grp * 8, tlo, thi);
      STAMP(4);
      bar();
      put(0, W[0]);
      fetch(5, W[0]);
      bar();
      put(1, W[1]); put(2, W[2]);
      if (lw == 0) poll(rc, myflag() + 128, tlo, thi);
      bar();
      bar();
      put(1, W[0]);
      bar();
      if (b == 0 && lw == 1) {
#pragma nounroll
        for (int i = 0; i < 4; ++i) poll(rc, WS_FLAG + i * 1024 + g * 256 + (lane & 8) * 16 + (lane & 7) * 8, tlo, thi);
        st_ctl(rc, lane, 0, tlo, thi);
      }
    } else {
      issue2(2);
      bar();
      fetch(3, W[0]);
      if (NF0 > 1) fetch(4, W[1]);
      STAMP(2);
      if (NF0 > 1) waitv<24>(); else waitv<12>();
      bar();
      if (SPLITPOLL && lw == 1) poll(rc, myflag(), tlo, thi);
      if (NF0 <= 1) fetch(4, W[1]);
      bar();
      put(1, W[0]);
      fetch(5, W[2]);
      bar();
      put(2, W[1]);
      if (lw == 0) poll(rc, myflag() + 128, tlo, thi);
      bar();
      bar();
      put(1, W[2]);
      bar();
    }
    STAMP(3);
    STAMP_FLUSH();
    return;
  }

  const bf8 one = ones8();
  {
    if (w == 1) *(LAS bf8*)(lds + AUX_ONES + lane * 16) = one;
    if (w == 2) {
      f4 mc, mp; u4 bm;
#pragma unroll
      for (int j = 0; j < 4; ++j) {
        int d = c16 - 4 * g - j;
        mc[j] = (unsigned)d < 8u ? 0.f : -1e30f;
        mp[j] = d < -8 ? 0.f : -1e30f;
      }
#pragma unroll
      for (int j = 0; j < 2; ++j) {
        int key = 4 * g + 2 * j;
        bm[j] = (key <= c16 ? 0xffffu : 0u) | (key + 1 <= c16 ? 0xffff0000u : 0u);
        bm[2 + j] = ~bm[j];
      }
      LAS char* p = lds + AUX_MASK + lane * 16;
      *(LAS f4*)p = mc; *(LAS f4*)(p + 1024) = mp; *(LAS f4*)(p + 2048) = f4{-1e30f, -1e30f, -1e30f, -1e30f}; *(LAS u4*)(p + 3072) = bm;
    }
  }
  const int nd = RB >= 4 ? (RB == 8 ? 8 : 4) : 0;
  const bool dbl = w < nd, idle = w >= 2 * RB - nd;
  const int part = w & 1;
  const int tb = tb0 + (dbl ? (w & ~1) : nd + ((w - nd) >> 1));
  const rsrc_t rsa = mkrs(actp + (size_t)tb * ACT_TB, idle ? 0 : ACT_TB), rsb = mkrs(actp + (size_t)(tb + 1) * ACT_TB, dbl ? ACT_TB : 0);
  u4 R[2][6];
  u2 bias2[2];
  auto xoff = [&](int ln) INL { return (unsigned)((tb * 16 + (ln & 15)) * 640 + ct * 64 + part * 32 + (ln & 16) + (ln >> 5) * 8) * 2; };
  auto first_acts = [&]() INL {
    if (idle) return;
    int ln = lane; PIN1(ln);
    const rsrc_t rb2 = mkrs(b2 + ct * 64 + part * 32, 64);
    bias2[0] = __builtin_amdgcn_raw_buffer_load_b64(rb2, (ln >> 4) * 8, 0, 0);
    bias2[1] = __builtin_amdgcn_raw_buffer_load_b64(rb2, (ln >> 4) * 8, 32, 0);
    SCHED();
    const unsigned va = ln * 16 + part * 6144 + gphys(0) * 12288;
    if (dbl) {
#pragma unroll
      for (int ks = 0; ks < 6; ++ks) {
        R[0][ks] = __builtin_amdgcn_raw_buffer_load_b128(rsa, va, ks * 1024, 16);
        R[1][ks] = __builtin_amdgcn_raw_buffer_load_b128(rsb, va, ks * 1024, 16);
        SCHED();
      }
    } else {
#pragma unroll
      for (int ks = 0; ks < 6; ++ks) { R[0][ks] = __builtin_amdgcn_raw_buffer_load_b128(rsa, va, ks * 1024, 16); SCHED(); }
    }
  };

  if (isattn) {
    const int tau = w >> 2, h = w & 3;
    const int hrow = c16 ^ (tau ? 0 : 8);
    auto xaddr = [&](int r) INL { return (r >> 3) * 1024 + (r & 7) * 64 + ((g ^ ((-(r >> 2)) & 3)) << 4); };
    int sg[3];
#pragma unroll
    for (int j = 0; j < 3; ++j) sg[j] = (j + h) % 3;
    const int wrow = ldsb + (16 * (g >> 1) + 4 * (g & 1) + q4) * 480;
    const int hm = c16 != 0;
    int px = ldsb + XOFF_A + 2048 + tau * 2048 + xaddr(c16), pxo = px + h * 4096;
    int ph = ldsb + (hm ? XOFF_A + xaddr(hrow) : AUX_ONES + (xaddr(hrow) & 255));
    int pw = wrow + 40 * h + 8 * p4, pr[3];
#pragma unroll
    for (int j = 0; j < 3; ++j) pr[j] = wrow + 40 * p4 + 32 + sg[j] * 160;
    f4 Q[4], K[4], V[4], Qh, Kh, Vh, R5[3], X5;
#pragma unroll
    for (int i = 0; i < 4; ++i) { Q[i] = f4{0, 0, 0, 0}; K[i] = Q[i]; V[i] = Q[i]; }
    Qh = Kh = Vh = R5[0] = R5[1] = R5[2] = X5 = Q[0];
    bf8 xf[4], xh, xo;
    WF wq, wk, wv, w5[3];
    xh = xf[0] = xf[1] = xf[2] = xf[3] = asb(u4{0, 0, 0, 0});
    wk.lo = wk.hi = wv.lo = wv.hi = __builtin_bit_cast(bf4, u2{0, 0});
#define MM(C, A, B) do { C = mfma(A, B, C); PIN1(C); SCHED(); } while (0)
    auto partA = [&](auto ldc, int ox, int ow) INL {
      constexpr bool LD = decltype(ldc)::value;
      if (LD) { w5[0].lo = ldt(LP(pr[0]) + ow); w5[0].hi = ldt(LP(pr[0]) + ow + 3840); }
      MM(K[0], fr(wk), xf[0]);
      if (LD) xo = ldx(LP(pxo) + ox);
      MM(K[1], fr(wk), xf[1]);
      if (LD) w5[1].lo = ldt(LP(pr[1]) + ow);
      MM(K[2], fr(wk), xf[2]);
      if (LD) w5[1].hi = ldt(LP(pr[1]) + ow + 3840);
      MM(K[3], fr(wk), xf[3]);
      if (LD) w5[2].lo = ldt(LP(pr[2]) + ow);
      MM(Kh, fr(wk), xh);
      if (LD) w5[2].hi = ldt(LP(pr[2]) + ow + 3840);
      MM(V[0], xf[0], fr(wv));
      if (LD) wq.lo = ldt(LP(pw) + ow);
      MM(V[1], xf[1], fr(wv));
      if (LD) wq.hi = ldt(LP(pw) + ow + 3840);
      MM(V[2], xf[2], fr(wv));
      if (LD) xf[0] = ldx(LP(px) + ox);
      MM(V[3], xf[3], fr(wv));
      if (LD) xf[1] = ldx(LP(px) + ox + 4096);
      MM(Vh, xh, fr(wv));
    };
    auto partB = [&](int ks, int dsl) INL {
      const int ox = ks * 512, ow = ks * 15360;
      xf[2] = ldx(LP(px) + ox + 8192);
      MM(R5[0], fr(w5[0]), xo);
      xf[3] = ldx(LP(px) + ox + 12288);
      MM(R5[1], fr(w5[1]), xo);
      xh = ldx(LP(ph) + ox);
      MM(R5[2], fr(w5[2]), xo);
      wk.lo = ldt(LP(pw) + ow + 160);
      MM(Q[0], fr(wq), xf[0]);
      wk.hi = ldt(LP(pw) + ow + 160 + 3840);
      MM(Q[1], fr(wq), xf[1]);
      wv.lo = ldt(LP(pw) + ow + 320);
      MM(Q[2], fr(wq), xf[2]);
      wv.hi = ldt(LP(pw) + ow + 320 + 3840);
      MM(Q[3], fr(wq), xf[3]);
      if (ks) { pr[0] += dsl; pr[1] += dsl; pr[2] += dsl; pxo += dsl; PIN4(pr[0], pr[1], pr[2], pxo); }
      MM(X5, fr(w5[0]), xh);
      if (ks) { pw += dsl; px += dsl; ph += dsl * hm; PIN1(pw); PIN1(px); PIN1(ph); }
      MM(Qh, fr(wq), xh);
    };
    __builtin_amdgcn_s_waitcnt(0xC07F);
    int c3 = 0;
#pragma nounroll
    for (int s = 0; s < 10; ++s) {
      if (!(ABL & 8) || s == 0) bar();
      if (s == 0) STAMP(14);
      const int dsl = c3 == 2 ? -2 * SLOT : SLOT;
      c3 = c3 == 2 ? 0 : c3 + 1;
      partA(std::true_type{}, 0, 0);
      partB(0, 0);
      partA(std::true_type{}, 512, 15360);
      partB(1, dsl);
    }
    {
      int ln = lane; PIN1(ln);
      int a1 = AUX_CS + sg[0] * 320 + 64 + 5 * (ln & 48), dm = AUX_DUMMY + ln * 16;
      PIN1(a1); PIN1(dm);
      a1 = (ln & 15) ? dm : a1;
      *(LAS f4*)(lds + a1) = X5;
    }
    bar();
    partA(std::false_type{}, 0, 0);
    STAMP(1);
    int ln = lane; PIN1(ln);
    const int cc = ln & 15, g4 = (ln >> 4) * 4;
    LAS char* cb = lds + AUX_CS;
    LAS char* rb = lds + SC_REM + tau * 7680 + cc * 32 + g4 * 2;
    u2 Qp[4], Kp[5], Vp[5];
    f4 nm4[4], r4[4], nmh4, rh4;
    float csv;
    {
      float nm[4], rr[4];
      LAS char* sl = lds + AUX_NM + (16 + 16 * tau + cc) * 4;
      f4 c5[3];
      const float nmo = *(LAS float*)(sl + h * 128), ro = *(LAS float*)(sl + 576 + h * 128);
#pragma unroll
      for (int j = 0; j < 3; ++j) c5[j] = *(LAS f4*)(cb + sg[j] * 320 + 64 + 20 * g4);
      const int hr = cc ^ (tau ? 0 : 8);
      const float nmh = *(LAS float*)(lds + AUX_NM + hr * 4), rh = *(LAS float*)(lds + AUX_R + hr * 4);
#pragma unroll
      for (int i = 0; i < 4; ++i) { nm[i] = *(LAS float*)(sl + i * 128); rr[i] = *(LAS float*)(sl + 576 + i * 128); }
      csv = asf(__builtin_amdgcn_ds_bpermute(cc * 4, asu(Vh[0])));
      f4 csk, csq;
#pragma unroll
      for (int j = 0; j < 4; ++j) {
        csq[j] = asf(__builtin_amdgcn_update_dpp(0u, asu(Qh[j]), 0x150, 0xf, 0xf, true));
        csk[j] = asf(__builtin_amdgcn_update_dpp(0u, asu(Kh[j]), 0x150, 0xf, 0xf, true));
      }
      SCHED();
#pragma unroll
      for (int j = 0; j < 3; ++j) *(LAS u2*)(rb + sg[j] * 2560 + (h + 1) * 512) = pk4(R5[j] * ro + c5[j] * nmo);
      *(LAS u2*)(rb + sg[0] * 2560) = pk4(X5 * rh + c5[0] * nmh);
      SCHED();
      Kp[0] = pk4(Kh * rh + csk * nmh);
#pragma unroll
      for (int i = 0; i < 4; ++i) Kp[i + 1] = pk4(K[i] * rr[i] + csk * nm[i]);
      PIN4(Kp[1], Kp[2], Kp[3], Kp[4]); PIN1(Kp[0]);
      SCHED();
      LAS char* sr = lds + AUX_NM + (16 + 16 * tau + g4) * 4;
      const int h4 = g4 ^ (tau ? 0 : 8);
      nmh4 = *(LAS f4*)(lds + AUX_NM + h4 * 4); rh4 = *(LAS f4*)(lds + AUX_R + h4 * 4);
#pragma unroll
      for (int i = 0; i < 4; ++i) { nm4[i] = *(LAS f4*)(sr + i * 128); r4[i] = *(LAS f4*)(sr + 576 + i * 128); }
      SCHED();
#pragma unroll
      for (int i = 0; i < 4; ++i) Qp[i] = pk4(Q[i] * rr[i] + csq * nm[i]);
      PIN4(Qp[0], Qp[1], Qp[2], Qp[3]);
    }
    STAMP(4);
    bar();
    unsigned early;
    {
      LAS char* rt = lds + SC_REM + tau * 7680 + 5120 + ln * 8;
      LAS char* mk = lds + AUX_MASK + ln * 16;
      u2 K5p[5], V5p[5], q5[4];
#pragma unroll
      for (int i = 0; i < 5; ++i) {
        K5p[i] = *(volatile LAS u2*)(rb + 2560 + i * 512);
        if (i < 4) q5[i] = *(LAS u2*)(rb + (i + 1) * 512);
      }
      const f4 mcur = *(LAS f4*)mk, mprev = *(LAS f4*)(mk + 1024), mprev0 = *(LAS f4*)(mk + (panel == 0 ? 2048 : 1024));
      const u4 bm = *(LAS u4*)(mk + 3072);
#pragma unroll
      for (int i = 0; i < 5; ++i) V5p[i] = __builtin_bit_cast(u2, ldt(rt + i * 512));
      SCHED();
      Vp[0] = pk4(Vh * rh4 + nmh4 * csv);
#pragma unroll
      for (int i = 0; i < 4; ++i) Vp[i + 1] = pk4(V[i] * r4[i] + nm4[i] * csv);
      PIN4(Vp[1], Vp[2], Vp[3], Vp[4]); PIN1(Vp[0]);
      SCHED();
      unsigned qm = g4 == 4 * h ? ~0u : 0u;
      PIN1(qm);
      float cexp = 0.22360679774997896f * 1.4426950408889634f;
      PIN1(cexp);
      f4 sv[4];
      float mx[4];
      {
        f4 sc[4], sp[4];
#pragma unroll
        for (int i = 0; i < 4; ++i) {
          bf8 qb = cat(Qp[i], q5[i] & qm);
          sc[i] = mfma(cat(Kp[i + 1], K5p[i + 1]), qb, mcur);
          sp[i] = mfma(cat(Kp[i], K5p[i]), qb, i == 0 ? mprev0 : mprev);
        }
#pragma unroll
        for (int i = 0; i < 4; ++i) {
#pragma unroll
          for (int j = 0; j < 4; ++j) sv[i][j] = fmx(sc[i][j], sp[i][j]);
          mx[i] = fmx(fmx(sv[i][0], sv[i][1]), fmx(sv[i][2], sv[i][3]));
        }
      }
      {
        auto u = SWAP16(asu(mx[0]), asu(mx[1])), v = SWAP16(asu(mx[2]), asu(mx[3]));
        auto t = SWAP32(asu(fmx(asf(u[0]), asf(u[1]))), asu(fmx(asf(v[0]), asf(v[1]))));
        unsigned m = asu(fmx(asf(t[0]), asf(t[1])) * cexp);
        auto a = SWAP32(m, m);
        auto a0 = SWAP16(a[0], a[0]), a1 = SWAP16(a[1], a[1]);
        mx[0] = asf(a0[0]); mx[1] = asf(a0[1]); mx[2] = asf(a1[0]); mx[3] = asf(a1[1]);
      }
      bf8 pb[4];
#pragma unroll
      for (int i = 0; i < 4; ++i) {
        f4 pv;
#pragma unroll
        for (int j = 0; j < 4; ++j) pv[j] = __builtin_amdgcn_exp2f(__builtin_fmaf(sv[i][j], cexp, -mx[i]));
        u2 pp = pk4(pv);
        pb[i] = asb(u4{pp[0], pp[1], pp[0], pp[1]} & bm);
      }
      PIN4(pb[0], pb[1], pb[2], pb[3]);
      STAMP(6);
      early = rfl(*(volatile LAS unsigned*)(lds + AUX_RDY));
      if (early) first_acts();
      const int ob = SC_O + (2 * cc + tau) * OPITCH + 40 * h;
      int ob5 = g4 == 4 * h ? ob + 32 : SC_O + (2 * cc + tau) * OPITCH + 160;
      PIN1(ob5);
      const f4 zero = {0, 0, 0, 0};
#pragma unroll
      for (int i = 0; i < 4; ++i) {
        f4 l = mfma(one, pb[i], zero);
        f4 o = mfma(cat(Vp[i + 1], Vp[i]), pb[i], zero);
        f4 o5 = mfma(cat(V5p[i + 1], V5p[i]), pb[i], zero);
        float inv = __builtin_amdgcn_rcpf(l[0]);
        *(LAS u2*)(lds + ob + i * 32 * OPITCH + 2 * g4) = pk4(o * inv);
        *(LAS u2*)(lds + ob5 + i * 32 * OPITCH) = pk4(o5 * inv);
      }
    }
    STAMP(5);
    bar();
    if (!early) first_acts();
  } else {
    const int tq = w & 3, fh = w >> 2;
    const int rho = 4 * (g & 1) + q4;
    const int role = (tq + 2 * fh) & 3;
    int px = ldsb + XOFF_F + tq * 4096 + (c16 >> 3) * 1024 + (c16 & 7) * 64 + ((g ^ ((-(c16 >> 2)) & 3)) << 4);
    const int wl = (g >> 1) * 12288 + rho * 128 + (((p4 >> 1) ^ (rho & 6)) << 4) + (p4 & 1) * 8;
    int wb[6], pu[3];
    u2 bias[6];
#pragma unroll
    for (int u = 0; u < 3; ++u) {
      pu[u] = 3 * fh + (u + role) % 3;
      wb[2 * u] = ldsb + (wl ^ ((pu[u] & 1) << 6)) + (pu[u] >> 1) * 1024;
      wb[2 * u + 1] = ldsb + (wl ^ ((pu[u] & 1) << 6) ^ 32) + (pu[u] >> 1) * 1024;
      bias[2 * u] = *(const u2*)(b1 + grp * 192 + pu[u] * 32 + 4 * g);
      bias[2 * u + 1] = *(const u2*)(b1 + grp * 192 + pu[u] * 32 + 16 + 4 * g);
    }
    f4 acc[2][6], CS[2];
#pragma unroll
    for (int e = 0; e < 6; ++e) { acc[0][e] = f4{0, 0, 0, 0}; acc[1][e] = acc[0][e]; }
    CS[0] = CS[1] = acc[0][0];
    bf8 onev = one;
    PIN1(onev);
    bf8 xf[2][2];
    WF wf[2][6];
    xf[1][0] = xf[1][1] = asb(u4{0, 0, 0, 0});
#pragma unroll
    for (int e = 0; e < 6; ++e) wf[1][e].lo = wf[1][e].hi = __builtin_bit_cast(bf4, u2{0, 0});
    auto rd = [&](int lk, int n) INL {
      if (n == 2 || n == 3) xf[lk][n - 2] = ldx(LP(px) + lk * 512 + (n - 2) * 2048);
      else { const int q = n < 2 ? n : n - 2; if (q & 1) wf[lk][q >> 1].hi = ldt(LP(wb[q >> 1]) + lk * 3072 + 6144); else wf[lk][q >> 1].lo = ldt(LP(wb[q >> 1]) + lk * 3072); }
    };
    auto mm = [&](int ks, int m) INL {
      if (m == 2) MM(CS[0], fr(wf[ks][0]), onev);
      else if (m == 5) MM(CS[1], fr(wf[ks][1]), onev);
      else { const int q = m < 2 ? m : m < 5 ? m - 1 : m - 2; MM(acc[q & 1][q >> 1], fr(wf[ks][q >> 1]), xf[ks][q & 1]); }
    };
    constexpr int SPL = 6;
    __builtin_amdgcn_s_waitcnt(0xC07F);
    int c3 = 0;
#pragma nounroll
    for (int s = 0; s < 10; ++s) {
      if (!(ABL & 8) || s == 0) bar();
      if (s == 0) STAMP(14);
      const int dsl = c3 == 2 ? -2 * SLOT : SLOT;
      c3 = c3 == 2 ? 0 : c3 + 1;
#pragma unroll
      for (int i = 0; i < 28; ++i) {
        const int r0 = i < 3 ? 2 * i : i + 3, r1 = i < 3 ? r0 + 2 : r0 + 1;
#pragma unroll
        for (int r = r0; r < r1; ++r) if (r < 28) rd(r / 14, r % 14);
        if (i == 25) { wb[0] += dsl; wb[1] += dsl; PIN1(wb[0]); PIN1(wb[1]); }
        if (i == 26) { wb[2] += dsl; wb[3] += dsl; px += dsl; PIN1(wb[2]); PIN1(wb[3]); PIN1(px); }
        if (i == 27) { wb[4] += dsl; wb[5] += dsl; PIN1(wb[4]); PIN1(wb[5]); }
        if (i < 14 - SPL) mm(1, SPL + i);
        else if (i < 28 - SPL) mm(0, i - (14 - SPL));
        else mm(1, i - (28 - SPL));
      }
    }
    {
      int ln = lane; PIN1(ln);
      int a0 = AUX_CS + pu[0] * 128 + (ln & 48), dm = AUX_DUMMY + ln * 16;
      PIN1(a0); PIN1(dm);
      a0 = (ln & 15) ? dm : a0;
      *(LAS f4*)(lds + a0) = CS[0]; *(LAS f4*)(lds + a0 + 64) = CS[1];
    }
    bar();
#pragma unroll
    for (int m = SPL; m < 14; ++m) mm(1, m);
    STAMP(1);
    float nm[2], rr[2];
    f4 cb4[6];
#pragma unroll
    for (int i = 0; i < 2; ++i) { nm[i] = *(LAS float*)(lds + AUX_NM + (32 * tq + 16 * i + c16) * 4); rr[i] = *(LAS float*)(lds + AUX_R + (32 * tq + 16 * i + c16) * 4); }
#pragma unroll
    for (int e = 0; e < 6; ++e) cb4[e] = *(LAS f4*)(lds + AUX_CS + (pu[e >> 1] * 32 + (e & 1) * 16 + 4 * g) * 4);
#pragma unroll
    for (int i = 0; i < 2; ++i) {
      const rsrc_t rs = mkrs(actp + (size_t)(panel * 8 + 2 * tq + i) * ACT_TB + 6 * grp * 1024, 6144);
#pragma unroll
      for (int u = 0; u < 3; ++u) {
        u2 hp[2];
#pragma unroll
        for (int d = 0; d < 2; ++d) {
          const int e = 2 * u + d;
          f4 v = acc[i][e] * rr[i] + (cb4[e] * nm[i] + unpk4(bias[e]));
#pragma unroll
          for (int j = 0; j < 4; ++j) v[j] = v[j] * fmx(v[j], 0.f);
          hp[d] = pk4(v);
        }
        st128<true>(u4{hp[0][0], hp[0][1], hp[1][0], hp[1][1]}, rs, lane * 16, pu[u] * 1024);
        SCHED();
      }
    }
    STAMP(6);
    bar();
    const unsigned tlo = rfl(*(LAS unsigned*)(lds + AUX_TAG)), thi = rfl(*(LAS unsigned*)(lds + AUX_TAG + 4));
    waitv<0>();
    if (arrive(ldsb + AUX_CNT) == 7) st_ctl(rc, lane, WS_FLAG + panel * 256 + grp * 8, tlo, thi);
    STAMP(2);
    if (w == 7) poll(rc, myflag1(), tlo, thi);
    bar();
    first_acts();
  }

  asm volatile("" :: "v"(cpf));
  if (idle) {
    bar(); bar(); bar(); bar(); bar();
    STAMP_FLUSH();
    return;
  }
  auto phase2 = [&](auto nrc) INL {
    constexpr int NR = decltype(nrc)::value;
    STAMP(7);
    int ln = lane; PIN1(ln);
    const int rho = (ln >> 2) & 7;
    const int wl = ((ln >> 5) * 24576 + rho * 128 + ((((ln >> 1) & 1) ^ (rho & 6)) << 4) + (ln & 1) * 8 + part * 6144) ^ (part << 6);
    const unsigned va0 = ln * 16 + part * 6144;
    LAS char* xw = lds + w * 4096 + ln * 16;
    LAS char* xd = lds + (w ^ 1) * 4096 + ln * 16;
    bf8 wf[2][4];
#pragma unroll
    for (int c = 0; c < 4; ++c) { wf[0][c] = ldw(lds + SLOT + (wl ^ (c << 5)), 12288); SCHED(); }
    auto group = [&](auto lastc, int G, int slot, int nslot, f4 (&acc)[NR][4]) INL {
      constexpr bool LAST = decltype(lastc)::value;
      const unsigned va = va0 + gphys(G + 1) * 12288;
      const unsigned va3 = (G >= 3 && ln >= 32) ? OOB : va;
      SCHED();
#pragma unroll
      for (int ks = 0; ks < 6; ++ks) {
        if (ks == 5) {
          if (!LAST) bar();
          STAMP(8 + G);
        }
        LAS char* sp = ks == 5 ? lds + nslot : lds + slot + (ks + 1) * 1024;
#pragma unroll
        for (int c = 0; c < 4; ++c) {
          if (!LAST || ks < 5) wf[(ks + 1) & 1][c] = ldw(sp + (wl ^ (c << 5)), 12288);
#pragma unroll
          for (int r = 0; r < NR; ++r) { acc[r][c] = mfma(wf[ks & 1][c], asb(R[r][ks]), acc[r][c]); PIN1(acc[r][c]); }
          SCHED();
        }
        if (!LAST && !(ABL & 64)) {
          R[0][ks] = __builtin_amdgcn_raw_buffer_load_b128(rsa, (ks % 3 == 2) ? va3 : va, ks * 1024, 16);
          if (NR == 2) R[1][ks] = __builtin_amdgcn_raw_buffer_load_b128(rsb, (ks % 3 == 2) ? va3 : va, ks * 1024, 16);
          SCHED();
        }
      }
    };
    f4 F[NR][4], S[NR][4];
#pragma unroll
    for (int r = 0; r < NR; ++r)
#pragma unroll
      for (int c = 0; c < 4; ++c) F[r][c] = f4{0, 0, 0, 0};
    int slot = SLOT;
#pragma nounroll
    for (int G = 0; G < 4; ++G) {
      const int nslot = slot == 2 * SLOT ? 0 : slot + SLOT;
      group(std::false_type{}, G, slot, nslot, F);
      slot = nslot;
    }
#pragma unroll
    for (int r = 0; r < NR; ++r) {
      *(LAS f4*)(xw + r * 2048) = F[r][2]; *(LAS f4*)(xw + r * 2048 + 1024) = F[r][3];
#pragma unroll
      for (int c = 0; c < 4; ++c) S[r][c] = f4{0, 0, 0, 0};
    }
    group(std::false_type{}, 4, 2 * SLOT, SLOT, S);
    PIN1(bias2[0]); PIN1(bias2[1]);
#pragma unroll
    for (int r = 0; r < NR; ++r)
#pragma unroll
      for (int c = 0; c < 2; ++c) {
        f4 f = F[r][c] + *(LAS f4*)(xd + r * 2048 + c * 1024) + unpk4(bias2[c]);
#pragma unroll
        for (int j = 0; j < 4; ++j) F[r][c][j] = f[j] * __builtin_amdgcn_rcpf(1.f + __builtin_amdgcn_exp2f(f[j] * -1.4426950408889634f));
        PIN1(F[r][c]);
      }
    SCHED();
    u4 xr[NR];
#pragma unroll
    for (int r = 0; r < NR; ++r) xr[r] = __builtin_amdgcn_raw_buffer_load_b128(mkrs(x, 2048 * 1280), xoff(ln), r * 16 * 1280, 0);
    group(std::true_type{}, 5, SLOT, SLOT, S);
#pragma unroll
    for (int r = 0; r < NR; ++r) { *(LAS f4*)(xw + 2 * SLOT + r * 2048) = S[r][2]; *(LAS f4*)(xw + 2 * SLOT + r * 2048 + 1024) = S[r][3]; }
    bar();
    const rsrc_t ro = mkrs(out, 2048 * 1280);
#pragma unroll
    for (int r = 0; r < NR; ++r) {
      f4 sa = S[r][0] + *(LAS f4*)(xd + 2 * SLOT + r * 2048), sb = S[r][1] + *(LAS f4*)(xd + 2 * SLOT + r * 2048 + 1024);
      u2 ya = pk4(sa * F[r][0]), yb = pk4(sb * F[r][1]);
      auto t0 = SWAP16(ya[0], yb[0]), t1 = SWAP16(ya[1], yb[1]);
      u4 y = {t0[0], t1[0], t0[1], t1[1]}, o;
      PIN1(xr[r]);
#pragma unroll
      for (int d = 0; d < 4; ++d) o[d] = pk2(lo16(xr[r][d]) + lo16(y[d]), hi16(xr[r][d]) + hi16(y[d]));
      st128<false>(o, ro, xoff(ln), r * 16 * 1280);
    }
    STAMP(3);
    STAMP_FLUSH();
  };
  if (dbl) phase2(std::integral_constant<int, 2>{});
  else phase2(std::integral_constant<int, 1>{});
}

extern "C" void kernel_launch(void* const* d_in, const int* in_sizes, int n_in, void* d_out, int out_size, void* d_ws, size_t ws_size,
                              hipStream_t stream) {
  (void)in_sizes; (void)n_in; (void)out_size; (void)ws_size;
  (void)hipFuncSetAttribute((const void*)fused, hipFuncAttributeMaxDynamicSharedMemorySize, LDS_BYTES);
  fused<<<256, 768, LDS_BYTES, stream>>>((const u16*)d_in[0], (const u16*)d_in[1], (const u16*)d_in[2], (const u16*)d_in[4],
                                         (const u16*)d_in[5], (const u16*)d_in[6], (const u16*)d_in[7], (u16*)d_out, (char*)d_ws);
}
```
